```python
import torch

def _softmax(x, axis=-1):
    return torch.softmax(x, dim=axis)
def _gelu(x):
    return torch.nn.functional.gelu(x, approximate="tanh")
def _silu(x):
    return torch.nn.functional.silu(x)
def _sigmoid(x):
    return torch.sigmoid(x)
def _layernorm(x, axis=-1, eps=1e-5):
    m = x.mean(dim=axis, keepdim=True)
    v = x.var(dim=axis, keepdim=True, unbiased=False)
    return (x - m) / torch.sqrt(v + eps)
def _scatter_add(tgt, idx, src, axis=0):
    return tgt.clone().index_add_(axis, idx, src)
def _segment_sum(src, seg, n):
    out = torch.zeros((n,) + src.shape[1:], dtype=src.dtype)
    return out.index_add_(0, seg, src)
def _pad(x, pw):
    flat = tuple(v for pair in reversed(pw) for v in pair)
    return torch.nn.functional.pad(x, flat)

kernel_name = 'concat_heads__linear_attn_block__sliding_local_dilated__prompt'

def setup_inputs():

    _w = (lambda a: (lambda t: t.to(torch.bfloat16) if t.is_floating_point() else t)(torch.as_tensor(a)))
    def _A_setup():
        B, S, D = 1, 2048, 448
        return {
            "x": _w((lambda *s: torch.randn(*s, dtype=torch.bfloat16) * 0.05)(B, S, D)),
            "wqkv": _w((lambda *s: torch.randn(*s, dtype=torch.bfloat16) * 1.6)(D, 3 * D)),
            "wo": _w((lambda *s: torch.randn(*s, dtype=torch.bfloat16) * 0.8)(D, D)),
        }
    _A_in = _A_setup()

    def _B_setup():
        B, S, D = 1, 2048, 448
        return {
            "x": _w((lambda *s: torch.randn(*s, dtype=torch.bfloat16) * 0.2)(B, S, D)),
            "wqkv": _w((lambda *s: torch.randn(*s, dtype=torch.bfloat16) * 0.2)(D, 3 * D)),
            "wo": _w((lambda *s: torch.randn(*s, dtype=torch.bfloat16) * 0.05)(D, D)),
        }
    _B_in = _B_setup()

    return {
        "A_x": _A_in["x"],
        "A_wqkv": _A_in["wqkv"],
        "A_wo": _A_in["wo"],
        "B_x": _B_in["x"],
        "B_wqkv": _B_in["wqkv"],
        "B_wo": _B_in["wo"],
    }

def reference(A_x, A_wqkv, A_wo, B_x, B_wqkv, B_wo):
    def _A_ref(x, wqkv, wo):
        B, S, D = x.shape
        H = 16; Dh = D // H
        qkv = (lambda x, *d: x.permute(*d))((x @ wqkv).reshape(B, S, 3, H, Dh), 2, 0, 3, 1, 4)
        q, k, v = qkv[0], qkv[1], qkv[2]
        phi = lambda t: torch.where(t > 0, t + 1, torch.exp(t)) + 1e-6
        pq, pk = phi(q), phi(k)
        kv = torch.einsum("bhsd,bhse->bhde", pk, v)
        z = torch.einsum("bhsd->bhd", pk)
        num = torch.einsum("bhqd,bhde->bhqe", pq, kv)
        den = torch.einsum("bhqd,bhd->bhq", pq, z)[..., None] + 1e-6
        o = num / den
        return (lambda x, *d: x.permute(*d))(o, 0, 2, 1, 3).reshape(B, S, D) @ wo

    def _B_ref(x, wqkv, wo):
        B, S, D = x.shape
        H = 16; Dh = D // H
        W, dil = 8, 2
        qkv = (lambda x, *d: x.permute(*d))((x @ wqkv).reshape(B, S, 3, H, Dh), 2, 0, 3, 1, 4)
        q, k, v = qkv[0], qkv[1], qkv[2]
        scale = Dh ** -0.5
        pos = torch.arange(S)
        off = torch.arange(W) * dil
        idx = pos[:, None] - off[None, :]
        mask = idx >= 0
        idx_c = torch.where(mask, idx, 0).reshape(-1)
        kg = k[:, :, idx_c, :].reshape(B, H, S, W, Dh)
        vg = v[:, :, idx_c, :].reshape(B, H, S, W, Dh)
        logits = torch.einsum("bhsd,bhswd->bhsw", q, kg) * scale
        logits = torch.where(mask[None, None, :, :], logits, -1e9)
        attn = _softmax(logits, axis=-1)
        o = torch.einsum("bhsw,bhswd->bhsd", attn, vg)
        return (lambda x, *d: x.permute(*d))(o, 0, 2, 1, 3).reshape(B, S, D) @ wo

    a = _A_ref(A_x, A_wqkv, A_wo)
    b = _B_ref(B_x, B_wqkv, B_wo)
    return torch.concatenate([a, b], axis=-1)
```

```cpp
#include <hip/hip_runtime.h>
#include <hip/hip_bf16.h>
#include <stdint.h>

typedef __bf16 bf16;
typedef __attribute__((ext_vector_type(8))) __bf16 bf16x8;
typedef __attribute__((ext_vector_type(4))) __bf16 bf16x4;
typedef __attribute__((ext_vector_type(4))) float f32x4;
#define LAS __attribute__((address_space(3)))
#define DI __device__ __forceinline__

constexpr int SEQ = 2048, DM = 448, NQKV = 1344;

DI float bfr(float x) { return (float)(__bf16)x; }

DI void glds16(const void* gsrc, unsigned lds_dst) {
  unsigned keep;
  asm volatile("s_mov_b32 %0, m0\n\ts_mov_b32 m0, %2\n\ts_nop 0\n\tglobal_load_lds_dwordx4 %1, off\n\ts_mov_b32 m0, %0"
               : "=&s"(keep) : "v"(gsrc), "s"(lds_dst) : "memory");
}
#define WAIT_VM(n) asm volatile("s_waitcnt vmcnt(" #n ")" ::: "memory")
#define BARRIER() do { asm volatile("s_waitcnt lgkmcnt(0)" ::: "memory"); __builtin_amdgcn_s_barrier(); asm volatile("" ::: "memory"); } while (0)

DI bf16x8 tr_frag(const char* p, int hi_off) {
  bf16x4 lo = __builtin_amdgcn_ds_read_tr16_b64_v4bf16((LAS bf16x4*)(p));
  bf16x4 hi = __builtin_amdgcn_ds_read_tr16_b64_v4bf16((LAS bf16x4*)(p + hi_off));
  return __builtin_shufflevector(lo, hi, 0, 1, 2, 3, 4, 5, 6, 7);
}
DI f32x4 mfma16(bf16x8 a, bf16x8 b, f32x4 c) { return __builtin_amdgcn_mfma_f32_16x16x32_bf16(a, b, c, 0, 0, 0); }

DI bf16x8 pack8(f32x4 a, f32x4 b) {
  bf16x8 r;
#pragma unroll
  for (int i = 0; i < 4; ++i) { r[i] = (__bf16)a[i]; r[4 + i] = (__bf16)b[i]; }
  return r;
}
DI float phi_f(float t) {
  float r = t > 0.f ? bfr(t + 1.0f) : bfr(__expf(t));
  return bfr(r + 1e-6f);
}
DI int pi_inv(int lr) { return lr < 16 ? 8 * (lr >> 2) + (lr & 3) : 8 * ((lr - 16) >> 2) + 4 + (lr & 3); }

constexpr int K1_XB = 144 * 128;
constexpr int K1_WB = 64 * 352;
constexpr int K1_STAGE = K1_XB + K1_WB;
constexpr int K1_LDS = 3 * K1_STAGE;

template <int BR>
DI void k1_body(const bf16* __restrict__ x, const bf16* __restrict__ w, bf16* __restrict__ outq,
                float* __restrict__ part, int rt, int hg, char* lds) {
  const int tid = threadIdx.x, lane = tid & 63;
  const int wv = __builtin_amdgcn_readfirstlane(tid >> 6);
  const int rh = wv >> 1, hh = wv & 1;
  const int g = lane >> 4, c16 = lane & 15;
  const int s0 = rt * 128;
  const unsigned ldsbase = (unsigned)(size_t)lds;

  const char* src[10];
  unsigned dst[10];
  long strd0;
#pragma unroll
  for (int i = 0; i < 4; ++i) {
    int xi = 2 + wv + 4 * i;
    int row = xi * 8 + (lane >> 3), pos = lane & 7;
    int grow = s0 - 16 + row;
    src[i] = (const char*)(x + (long)grow * DM + ((pos ^ (row & 7)) * 8));
    dst[i] = xi * 1024;
  }
#pragma unroll
  for (int i = 0; i < 6; ++i) {
    int j = wv + 4 * i;
    if (i == 0 && j < 2) {
      int row = j * 8 + (lane >> 3), pos = lane & 7;
      int grow = s0 - 16 + row; grow = grow < 0 ? 0 : grow;
      src[4] = (const char*)(x + (long)grow * DM + ((pos ^ (row & 7)) * 8));
      dst[4] = j * 1024;
      strd0 = 128;
    } else {
      int wi = j - 2;
      int c = wi * 64 + lane;
      int lrow = c / 22, pos = c - lrow * 22;
      pos = pos > 20 ? 20 : pos;
      int k = (lrow & 32) + pi_inv(lrow & 31);
      int seg = pos / 7, cc = pos - seg * 7;
      src[4 + i] = (const char*)(w + (long)k * NQKV + seg * DM + hg * 56 + cc * 8);
      dst[4 + i] = K1_XB + wi * 1024;
      if (i == 0) strd0 = 64L * NQKV * 2;
    }
  }
  auto issue = [&](int st, int buf) {
    unsigned b = ldsbase + buf * K1_STAGE;
#pragma unroll
    for (int i = 0; i < 4; ++i) glds16(src[i] + st * 128, b + dst[i]);
    glds16(src[4] + st * strd0, b + dst[4]);
#pragma unroll
    for (int i = 5; i < 10; ++i) glds16(src[i] + (long)st * (64L * NQKV * 2), b + dst[i]);
  };

  constexpr int T0 = BR ? 0 : 1;
  f32x4 aq[4][2], ak[5][2], av[5][2];
#pragma unroll
  for (int t = 0; t < 5; ++t)
#pragma unroll
    for (int c = 0; c < 2; ++c) {
      if (t < 4) aq[t][c] = f32x4{0, 0, 0, 0};
      ak[t][c] = f32x4{0, 0, 0, 0}; av[t][c] = f32x4{0, 0, 0, 0};
    }

  issue(0, 0);
  issue(1, 1);
  const int xoff = (rh * 64 + c16) * 128;
  const int woff = K1_XB + (4 * g + (c16 >> 2)) * 352 + hh * 56 + (c16 & 3) * 8;
#pragma unroll 1
  for (int st = 0; st < 7; ++st) {
    if (st < 6) WAIT_VM(10); else WAIT_VM(0);
    BARRIER();
    if (st + 2 < 7) issue(st + 2, (st + 2) % 3);
    const char* sb = lds + (st % 3) * K1_STAGE;
#pragma unroll
    for (int ks = 0; ks < 2; ++ks) {
      bf16x8 xf[5], wf[6];
      const int xs = ((ks * 4 + g) ^ (c16 & 7)) << 4;
#pragma unroll
      for (int t = T0; t < 5; ++t) xf[t] = *(const bf16x8*)(sb + xoff + t * 2048 + xs);
#pragma unroll
      for (int ct = 0; ct < 6; ++ct)
        wf[ct] = tr_frag(sb + woff + ks * 32 * 352 + (ct >> 1) * 112 + (ct & 1) * 32, 16 * 352);
#pragma unroll
      for (int t = T0; t < 5; ++t) {
#pragma unroll
        for (int c = 0; c < 2; ++c) {
          if (t >= 1) aq[t - 1][c] = mfma16(wf[c], xf[t], aq[t - 1][c]);
          if (BR) ak[t][c] = mfma16(wf[2 + c], xf[t], ak[t][c]);
          else    ak[t][c] = mfma16(xf[t], wf[2 + c], ak[t][c]);
          av[t][c] = mfma16(xf[t], wf[4 + c], av[t][c]);
        }
      }
    }
  }

  const int h = hg * 2 + hh;
  const int srow = s0 + rh * 64 + c16;
  if (BR) {
    const float scale = 0.18898223650461362f;
    bf16x8 kp[5];
#pragma unroll
    for (int t = 0; t < 5; ++t) {
      f32x4 hi = ak[t][1];
      if (g == 3) hi = f32x4{0, 0, 0, 0};
      kp[t] = pack8(ak[t][0], hi);
    }
#pragma unroll
    for (int t = 0; t < 4; ++t) {
      f32x4 hi = aq[t][1];
      if (g == 3) hi = f32x4{0, 0, 0, 0};
      bf16x8 qp = pack8(aq[t][0], hi);
      f32x4 sp = mfma16(kp[t], qp, f32x4{0, 0, 0, 0});
      f32x4 sc = mfma16(kp[t + 1], qp, f32x4{0, 0, 0, 0});
      const bool prev_ok = (s0 + rh * 64 + t * 16) > 0;
      float lg[8]; bool ok[8];
      float m = -3.0e38f;
#pragma unroll
      for (int j = 0; j < 4; ++j) {
        int dp = c16 + 16 - 4 * g - j, dc = c16 - 4 * g - j;
        ok[j] = prev_ok && ((dp & 1) == 0) && dp <= 14;
        ok[4 + j] = dc >= 0 && ((dc & 1) == 0);
        lg[j] = bfr(bfr(sp[j]) * scale);
        lg[4 + j] = bfr(bfr(sc[j]) * scale);
        if (ok[j]) m = fmaxf(m, lg[j]);
        if (ok[4 + j]) m = fmaxf(m, lg[4 + j]);
      }
      m = fmaxf(m, __shfl_xor(m, 16));
      m = fmaxf(m, __shfl_xor(m, 32));
      float p[8], sum = 0.f;
#pragma unroll
      for (int j = 0; j < 8; ++j) { p[j] = ok[j] ? __expf(lg[j] - m) : 0.f; sum += p[j]; }
      sum += __shfl_xor(sum, 16);
      sum += __shfl_xor(sum, 32);
      const float inv = 1.0f / sum;
      bf16x8 pp;
#pragma unroll
      for (int j = 0; j < 8; ++j) pp[j] = (__bf16)(p[j] * inv);
#pragma unroll
      for (int et = 0; et < 2; ++et) {
        bf16x8 vp = pack8(av[t][et], av[t + 1][et]);
        f32x4 o = mfma16(vp, pp, f32x4{0, 0, 0, 0});
        bf16x4 ob;
#pragma unroll
        for (int j = 0; j < 4; ++j) ob[j] = (__bf16)o[j];
        if (et == 0 || g < 3)
          *(bf16x4*)(outq + (long)(srow + 16 * t) * DM + h * 28 + 16 * et + 4 * g) = ob;
      }
    }
  } else {
#pragma unroll
    for (int t = 0; t < 4; ++t)
#pragma unroll
      for (int c = 0; c < 2; ++c) {
        bf16x4 ob;
#pragma unroll
        for (int j = 0; j < 4; ++j) ob[j] = (__bf16)phi_f(bfr(aq[t][c][j]));
        if (c == 0 || g < 3)
          *(bf16x4*)(outq + (long)(srow + 16 * t) * DM + h * 28 + 16 * c + 4 * g) = ob;
      }
#pragma unroll
    for (int t = 1; t < 5; ++t) {
#pragma unroll
      for (int j = 0; j < 4; ++j) {
        ak[t][0][j] = phi_f(bfr(ak[t][0][j]));
        ak[t][1][j] = phi_f(bfr(ak[t][1][j]));
        if (c16 >= 12) av[t][1][j] = (c16 == 12) ? 1.0f : 0.0f;
      }
    }
    f32x4 kv[2][2];
#pragma unroll
    for (int dc = 0; dc < 2; ++dc)
#pragma unroll
      for (int ec = 0; ec < 2; ++ec) {
        f32x4 a = f32x4{0, 0, 0, 0};
        a = mfma16(pack8(ak[1][dc], ak[2][dc]), pack8(av[1][ec], av[2][ec]), a);
        a = mfma16(pack8(ak[3][dc], ak[4][dc]), pack8(av[3][ec], av[4][ec]), a);
        kv[dc][ec] = a;
      }
    BARRIER();
    f32x4* ex = (f32x4*)(lds) + hh * 256 + lane;
    if (rh == 1) {
#pragma unroll
      for (int i = 0; i < 4; ++i) ex[i * 64] = kv[i >> 1][i & 1];
    }
    BARRIER();
    if (rh == 0) {
      float* pb = part + ((long)rt * 16 + h) * 1024;
#pragma unroll
      for (int i = 0; i < 4; ++i) {
        f32x4 o = kv[i >> 1][i & 1] + ex[i * 64];
        *(f32x4*)(pb + (16 * (i & 1) + c16) * 32 + 16 * (i >> 1) + 4 * g) = o;
      }
    }
  }
}

__global__ __launch_bounds__(256, 1) void k1_qkv(const bf16* __restrict__ xa, const bf16* __restrict__ wa,
                                                 const bf16* __restrict__ xb, const bf16* __restrict__ wb,
                                                 bf16* __restrict__ pq, bf16* __restrict__ ob,
                                                 float* __restrict__ part) {
  extern __shared__ __attribute__((aligned(16))) char lds[];
  const int b = blockIdx.x;
  const int hg = b & 7, idx = b >> 3;
  const int br = idx & 1, rt = idx >> 1;
  if (br == 0) k1_body<0>(xa, wa, pq, part, rt, hg, lds);
  else         k1_body<1>(xb, wb, ob, part, rt, hg, lds);
}

constexpr int OP_OB = 7 * 64 * 128;
constexpr int OP_WB = 448 * 224;
constexpr int OP_LDS = OP_OB + OP_WB;

DI void op_issue_w(const bf16* __restrict__ wo, int n0, int wv, int lane, unsigned ldsbase) {
  int off[7];
#pragma unroll
  for (int i = 0; i < 7; ++i) {
    int c = i * 64 + lane;
    int lrow = c / 14, pos = c - lrow * 14;
    off[i] = (pi_inv(lrow) * DM + n0 + pos * 8) * 2;
  }
#pragma unroll
  for (int r = 0; r < 4; ++r) {
    int kb = wv + 4 * r;
    if (kb < 14) {
      const char* base = (const char*)wo + (long)kb * 32 * DM * 2;
#pragma unroll
      for (int i = 0; i < 7; ++i) glds16(base + off[i], ldsbase + OP_OB + (kb * 7 + i) * 1024);
    }
  }
}

template <int NC>
DI void op_compute(const char* lds, bf16* __restrict__ out, int m0, int ncol0, int ct0, int rbase, int lane) {
  const int g = lane >> 4, c16 = lane & 15;
  f32x4 acc[2][NC];
#pragma unroll
  for (int r = 0; r < 2; ++r)
#pragma unroll
    for (int c = 0; c < NC; ++c) acc[r][c] = f32x4{0, 0, 0, 0};
  const int ooff = (rbase + c16) * 128;
  const int woff = OP_OB + (4 * g + (c16 >> 2)) * 224 + ct0 * 32 + (c16 & 3) * 8;
#pragma unroll
  for (int ks = 0; ks < 14; ++ks) {
    bf16x8 of[2], wf[NC];
    const int xs = (ks >> 1) * 8192 + ((((ks & 1) * 4 + g) ^ (c16 & 7)) << 4);
#pragma unroll
    for (int r = 0; r < 2; ++r) of[r] = *(const bf16x8*)(lds + ooff + r * 2048 + xs);
#pragma unroll
    for (int c = 0; c < NC; ++c) wf[c] = tr_frag(lds + woff + ks * 32 * 224 + c * 32, 16 * 224);
#pragma unroll
    for (int r = 0; r < 2; ++r)
#pragma unroll
      for (int c = 0; c < NC; ++c) acc[r][c] = mfma16(wf[c], of[r], acc[r][c]);
  }
#pragma unroll
  for (int r = 0; r < 2; ++r)
#pragma unroll
    for (int c = 0; c < NC; ++c) {
      bf16x4 ob;
#pragma unroll
      for (int j = 0; j < 4; ++j) ob[j] = (__bf16)acc[r][c][j];
      *(bf16x4*)(out + (long)(m0 + rbase + r * 16 + c16) * (2 * DM) + ncol0 + (ct0 + c) * 16 + 4 * g) = ob;
    }
}

__global__ __launch_bounds__(256, 1) void k2_outb(const bf16* __restrict__ ob, const bf16* __restrict__ wo,
                                                  const float* __restrict__ part, bf16* __restrict__ kvt,
                                                  bf16* __restrict__ out) {
  extern __shared__ __attribute__((aligned(16))) char lds[];
  const int tid = threadIdx.x, lane = tid & 63;
  const int wv = __builtin_amdgcn_readfirstlane(tid >> 6);
  const int b = blockIdx.x;
  if (b >= 128) {
    const int h = b - 128;
    const float* p = part + (long)h * 1024 + tid * 4;
    f32x4 s = *(const f32x4*)p;
#pragma unroll
    for (int i = 1; i < 16; ++i) s += *(const f32x4*)(p + (long)i * 16 * 1024);
    const int e = tid >> 3, d = (tid & 7) * 4;
    bf16x4 r;
#pragma unroll
    for (int j = 0; j < 4; ++j) r[j] = (__bf16)((e <= 28 && d + j < 28) ? s[j] : 0.f);
    *(bf16x4*)(kvt + h * 1024 + tid * 4) = r;
    return;
  }
  const unsigned ldsbase = (unsigned)(size_t)lds;
  const int nt = b & 3, mt = b >> 2;
  const int m0 = mt * 64, n0 = nt * 112;
  op_issue_w(wo, n0, wv, lane, ldsbase);
  {
    const int rsub = lane >> 3, pos = lane & 7;
#pragma unroll
    for (int i = 0; i < 14; ++i) {
      int oi = wv + 4 * i;
      int kc = oi >> 3, row = (oi & 7) * 8 + rsub;
      glds16(ob + (long)(m0 + row) * DM + kc * 64 + ((pos ^ (row & 7)) * 8), ldsbase + oi * 1024);
    }
  }
  WAIT_VM(0);
  BARRIER();
  if (wv < 2) op_compute<4>(lds, out, m0, DM + n0, 0, (wv & 1) * 32, lane);
  else        op_compute<3>(lds, out, m0, DM + n0, 4, (wv & 1) * 32, lane);
}

__global__ __launch_bounds__(256, 1) void k3_outa(const bf16* __restrict__ pq, const bf16* __restrict__ wo,
                                                  const bf16* __restrict__ kvt, bf16* __restrict__ out) {
  extern __shared__ __attribute__((aligned(16))) char lds[];
  const int tid = threadIdx.x, lane = tid & 63;
  const int wv = __builtin_amdgcn_readfirstlane(tid >> 6);
  const int g = lane >> 4, c16 = lane & 15;
  const int b = blockIdx.x;
  const unsigned ldsbase = (unsigned)(size_t)lds;
  const int nt = b & 3, mt = b >> 2;
  const int m0 = mt * 64, n0 = nt * 112;
  op_issue_w(wo, n0, wv, lane, ldsbase);
#pragma unroll
  for (int hi = 0; hi < 4; ++hi) {
    const int h = wv * 4 + hi;
    bf16x8 kf0 = *(const bf16x8*)(kvt + h * 1024 + c16 * 32 + 8 * g);
    bf16x8 kf1 = *(const bf16x8*)(kvt + h * 1024 + (16 + c16) * 32 + 8 * g);
#pragma unroll
    for (int t = 0; t < 4; ++t) {
      const bf16* qp = pq + (long)(m0 + 16 * t + c16) * DM + h * 28 + 8 * g;
      bf16x4 lo = *(const bf16x4*)qp;
      bf16x4 hi4;
      if (g < 3) hi4 = *(const bf16x4*)(qp + 4);
      else { hi4[0] = (__bf16)0.f; hi4[1] = (__bf16)0.f; hi4[2] = (__bf16)0.f; hi4[3] = (__bf16)0.f; }
      bf16x8 qf = __builtin_shufflevector(lo, hi4, 0, 1, 2, 3, 4, 5, 6, 7);
      f32x4 n0v = mfma16(kf0, qf, f32x4{0, 0, 0, 0});
      f32x4 n1v = mfma16(kf1, qf, f32x4{0, 0, 0, 0});
      float den = __shfl(n1v[0], 48 + c16);
      den = bfr(bfr(den) + 1e-6f);
      const float inv = 1.0f / den;
      bf16x4 o0, o1;
#pragma unroll
      for (int j = 0; j < 4; ++j) {
        o0[j] = (__bf16)(bfr(n0v[j]) * inv);
        o1[j] = (__bf16)(bfr(n1v[j]) * inv);
      }
      const int row = 16 * t + c16;
      {
        int col = h * 28 + 4 * g;
        *(bf16x4*)(lds + (col >> 6) * 8192 + row * 128 + ((((col & 63) >> 3) ^ (row & 7)) << 4) + (col & 7) * 2) = o0;
      }
      if (g < 3) {
        int col = h * 28 + 16 + 4 * g;
        *(bf16x4*)(lds + (col >> 6) * 8192 + row * 128 + ((((col & 63) >> 3) ^ (row & 7)) << 4) + (col & 7) * 2) = o1;
      }
    }
  }
  WAIT_VM(0);
  BARRIER();
  if (wv < 2) op_compute<4>(lds, out, m0, n0, 0, (wv & 1) * 32, lane);
  else        op_compute<3>(lds, out, m0, n0, 4, (wv & 1) * 32, lane);
}

extern "C" void kernel_launch(void* const* d_in, const int* in_sizes, int n_in,
                              void* d_out, int out_size, void* d_ws, size_t ws_size,
                              hipStream_t stream) {
  const bf16* xa = (const bf16*)d_in[0];
  const bf16* wqa = (const bf16*)d_in[1];
  const bf16* woa = (const bf16*)d_in[2];
  const bf16* xb = (const bf16*)d_in[3];
  const bf16* wqb = (const bf16*)d_in[4];
  const bf16* wob = (const bf16*)d_in[5];
  bf16* out = (bf16*)d_out;
  char* ws = (char*)d_ws;
  bf16* pq = (bf16*)ws;
  bf16* ob = (bf16*)(ws + 2 * 1024 * 1024);
  float* part = (float*)(ws + 4 * 1024 * 1024);
  bf16* kvt = (bf16*)(ws + 6 * 1024 * 1024);

  hipFuncSetAttribute((const void*)k1_qkv, hipFuncAttributeMaxDynamicSharedMemorySize, K1_LDS);
  hipFuncSetAttribute((const void*)k2_outb, hipFuncAttributeMaxDynamicSharedMemorySize, OP_LDS);
  hipFuncSetAttribute((const void*)k3_outa, hipFuncAttributeMaxDynamicSharedMemorySize, OP_LDS);

  k1_qkv<<<256, 256, K1_LDS, stream>>>(xa, wqa, xb, wqb, pq, ob, part);
  k2_outb<<<144, 256, OP_LDS, stream>>>(ob, wob, part, kvt, out);
  k3_outa<<<128, 256, OP_LDS, stream>>>(pq, woa, kvt, out);
}
```

```cpp
#include <hip/hip_runtime.h>
#include <hip/hip_bf16.h>
#include <stdint.h>

typedef __bf16 bf16;
typedef __attribute__((ext_vector_type(8))) __bf16 bf16x8;
typedef __attribute__((ext_vector_type(4))) __bf16 bf16x4;
typedef __attribute__((ext_vector_type(4))) float f32x4;
#define LAS __attribute__((address_space(3)))
#define DI __device__ __forceinline__

#define STAMP(k,i) do {} while (0)
constexpr int SEQ = 2048, DM = 448, NQKV = 1344;

DI float bfr(float x) { return (float)(__bf16)x; }

DI void glds16(const void* gsrc, unsigned lds_dst) {
  unsigned keep;
  asm volatile("s_mov_b32 %0, m0\n\ts_mov_b32 m0, %2\n\ts_nop 0\n\tglobal_load_lds_dwordx4 %1, off\n\ts_mov_b32 m0, %0"
               : "=&s"(keep) : "v"(gsrc), "s"(lds_dst) : "memory");
}
#define WAIT_VM(n) asm volatile("s_waitcnt vmcnt(" #n ")" ::: "memory")
#define BARRIER() do { asm volatile("s_waitcnt lgkmcnt(0)" ::: "memory"); __builtin_amdgcn_s_barrier(); asm volatile("" ::: "memory"); } while (0)

DI bf16x8 tr_frag(const char* p, int hi_off) {
  bf16x4 lo = __builtin_amdgcn_ds_read_tr16_b64_v4bf16((LAS bf16x4*)(p));
  bf16x4 hi = __builtin_amdgcn_ds_read_tr16_b64_v4bf16((LAS bf16x4*)(p + hi_off));
  return __builtin_shufflevector(lo, hi, 0, 1, 2, 3, 4, 5, 6, 7);
}
DI f32x4 mfma16(bf16x8 a, bf16x8 b, f32x4 c) { return __builtin_amdgcn_mfma_f32_16x16x32_bf16(a, b, c, 0, 0, 0); }

DI bf16x8 pack8(f32x4 a, f32x4 b) {
  bf16x8 r;
#pragma unroll
  for (int i = 0; i < 4; ++i) { r[i] = (__bf16)a[i]; r[4 + i] = (__bf16)b[i]; }
  return r;
}
DI float phi_f(float t) {
  float r = t > 0.f ? bfr(t + 1.0f) : bfr(__expf(t));
  return bfr(r + 1e-6f);
}
DI float xmax16_32(float m) {
  unsigned u = __float_as_uint(m);
  auto r = __builtin_amdgcn_permlane16_swap(u, u, false, false);
  float a = fmaxf(__uint_as_float(r[0]), __uint_as_float(r[1]));
  unsigned v = __float_as_uint(a);
  auto r2 = __builtin_amdgcn_permlane32_swap(v, v, false, false);
  return fmaxf(__uint_as_float(r2[0]), __uint_as_float(r2[1]));
}
DI float xsum16_32(float m) {
  unsigned u = __float_as_uint(m);
  auto r = __builtin_amdgcn_permlane16_swap(u, u, false, false);
  float a = __uint_as_float(r[0]) + __uint_as_float(r[1]);
  unsigned v = __float_as_uint(a);
  auto r2 = __builtin_amdgcn_permlane32_swap(v, v, false, false);
  return __uint_as_float(r2[0]) + __uint_as_float(r2[1]);
}
DI float bcast_row3(float x) {
  unsigned u = __float_as_uint(x);
  auto r = __builtin_amdgcn_permlane32_swap(u, u, false, false);
  unsigned v = r[1];
  auto r2 = __builtin_amdgcn_permlane16_swap(v, v, false, false);
  return __uint_as_float(r2[1]);
}
DI int pi_inv(int lr) { return lr < 16 ? 8 * (lr >> 2) + (lr & 3) : 8 * ((lr - 16) >> 2) + 4 + (lr & 3); }

constexpr int K1_XB = 144 * 128;
constexpr int K1_WB = 64 * 352;
constexpr int K1_STAGE = K1_XB + K1_WB;
constexpr int K1_LDS = 3 * K1_STAGE;

template <int BR>
DI void k1_body(const bf16* __restrict__ x, const bf16* __restrict__ w, bf16* __restrict__ outq,
                float* __restrict__ part, bf16* __restrict__ dummy, int rt, int hg, char* lds) {
  const int tid = threadIdx.x, lane = tid & 63;
  const int wv = __builtin_amdgcn_readfirstlane(tid >> 6);
  const int rh = wv >> 1, hh = wv & 1;
  const int g = lane >> 4, c16 = lane & 15;
  const int s0 = rt * 128;
  const unsigned ldsbase = (unsigned)(size_t)lds;

  const char* src[10];
  unsigned dst[10];
  long strd0;
#pragma unroll
  for (int i = 0; i < 4; ++i) {
    int xi = 2 + wv + 4 * i;
    int row = xi * 8 + (lane >> 3), pos = lane & 7;
    int grow = s0 - 16 + row;
    src[i] = (const char*)(x + (long)grow * DM + ((pos ^ (row & 7)) * 8));
    dst[i] = xi * 1024;
  }
#pragma unroll
  for (int i = 0; i < 6; ++i) {
    int j = wv + 4 * i;
    if (i == 0 && j < 2) {
      int row = j * 8 + (lane >> 3), pos = lane & 7;
      int grow = s0 - 16 + row; grow = grow < 0 ? 0 : grow;
      src[4] = (const char*)(x + (long)grow * DM + ((pos ^ (row & 7)) * 8));
      dst[4] = j * 1024;
      strd0 = 128;
    } else {
      int wi = j - 2;
      int c = wi * 64 + lane;
      int lrow = c / 22, pos = c - lrow * 22;
      pos = pos > 20 ? 20 : pos;
      int k = (lrow & 32) + pi_inv(lrow & 31);
      int seg = pos / 7, cc = pos - seg * 7;
      src[4 + i] = (const char*)(w + (long)k * NQKV + seg * DM + hg * 56 + cc * 8);
      dst[4 + i] = K1_XB + wi * 1024;
      if (i == 0) strd0 = 64L * NQKV * 2;
    }
  }
  auto issue = [&](int st, int buf) {
    unsigned b = ldsbase + buf * K1_STAGE;
#pragma unroll
    for (int i = 0; i < 4; ++i) glds16(src[i] + st * 128, b + dst[i]);
    glds16(src[4] + st * strd0, b + dst[4]);
#pragma unroll
    for (int i = 5; i < 10; ++i) glds16(src[i] + (long)st * (64L * NQKV * 2), b + dst[i]);
  };

  constexpr int T0 = BR ? 0 : 1;
  f32x4 aq[4][2], ak[5][2], av[5][2];
#pragma unroll
  for (int t = 0; t < 5; ++t)
#pragma unroll
    for (int c = 0; c < 2; ++c) {
      if (t < 4) aq[t][c] = f32x4{0, 0, 0, 0};
      ak[t][c] = f32x4{0, 0, 0, 0}; av[t][c] = f32x4{0, 0, 0, 0};
    }

  STAMP(0,0);
  issue(0, 0);
  issue(1, 1);
  STAMP(0,1);
  const int xoff = (rh * 64 + c16) * 128;
  const int woff = K1_XB + (4 * g + (c16 >> 2)) * 352 + hh * 56 + (c16 & 3) * 8;
#pragma unroll 1
  for (int st = 0; st < 7; ++st) {
    if (st < 6) WAIT_VM(10); else WAIT_VM(0);
    BARRIER();
    if (st == 0) STAMP(0,2);
    if (st == 3) STAMP(0,3);
    if (st + 2 < 7) issue(st + 2, (st + 2) % 3);
    const char* sb = lds + (st % 3) * K1_STAGE;
#pragma unroll
    for (int ks = 0; ks < 2; ++ks) {
      bf16x8 xf[5], wf[6];
      const int xs = ((ks * 4 + g) ^ (c16 & 7)) << 4;
#pragma unroll
      for (int t = T0; t < 5; ++t) xf[t] = *(const bf16x8*)(sb + xoff + t * 2048 + xs);
#pragma unroll
      for (int ct = 0; ct < 6; ++ct)
        wf[ct] = tr_frag(sb + woff + ks * 32 * 352 + (ct >> 1) * 112 + (ct & 1) * 32, 16 * 352);
#pragma unroll
      for (int t = T0; t < 5; ++t) {
#pragma unroll
        for (int c = 0; c < 2; ++c) {
          if (t >= 1) aq[t - 1][c] = mfma16(wf[c], xf[t], aq[t - 1][c]);
          if (BR) ak[t][c] = mfma16(wf[2 + c], xf[t], ak[t][c]);
          else    ak[t][c] = mfma16(xf[t], wf[2 + c], ak[t][c]);
          av[t][c] = mfma16(xf[t], wf[4 + c], av[t][c]);
        }
      }
    }
  }

  STAMP(0,4);
  const int h = hg * 2 + hh;
  const int srow = s0 + rh * 64 + c16;
  if (BR) {
    const float scale = 0.18898223650461362f;
    bf16x8 kp[5], qp[4];
#pragma unroll
    for (int t = 0; t < 5; ++t) {
      f32x4 hi = ak[t][1];
      if (g == 3) hi = f32x4{0, 0, 0, 0};
      kp[t] = pack8(ak[t][0], hi);
    }
#pragma unroll
    for (int t = 0; t < 4; ++t) {
      f32x4 hi = aq[t][1];
      if (g == 3) hi = f32x4{0, 0, 0, 0};
      qp[t] = pack8(aq[t][0], hi);
    }
    f32x4 sp[4], sc[4];
#pragma unroll
    for (int t = 0; t < 4; ++t) {
      sp[t] = mfma16(kp[t], qp[t], f32x4{0, 0, 0, 0});
      sc[t] = mfma16(kp[t + 1], qp[t], f32x4{0, 0, 0, 0});
    }
    bool okp[4], okc[4];
#pragma unroll
    for (int j = 0; j < 4; ++j) {
      int dp = c16 + 16 - 4 * g - j, dc = c16 - 4 * g - j;
      okp[j] = ((dp & 1) == 0) && dp <= 14;
      okc[j] = dc >= 0 && ((dc & 1) == 0);
    }
    const bool first = (s0 + rh * 64) == 0;
    float m[4];
#pragma unroll
    for (int t = 0; t < 4; ++t) {
      float mm = -3.0e38f;
#pragma unroll
      for (int j = 0; j < 4; ++j) {
        float lp = bfr(bfr(sp[t][j]) * scale), lc = bfr(bfr(sc[t][j]) * scale);
        bool op = okp[j] && !(t == 0 && first);
        sp[t][j] = op ? lp : -3.0e38f;
        sc[t][j] = okc[j] ? lc : -3.0e38f;
        mm = fmaxf(mm, fmaxf(sp[t][j], sc[t][j]));
      }
      m[t] = mm;
    }
#pragma unroll
    for (int t = 0; t < 4; ++t) m[t] = xmax16_32(m[t]);
    float sum[4];
#pragma unroll
    for (int t = 0; t < 4; ++t) {
      float ss = 0.f;
#pragma unroll
      for (int j = 0; j < 4; ++j) {
        bool op = okp[j] && !(t == 0 && first);
        sp[t][j] = op ? __expf(sp[t][j] - m[t]) : 0.f;
        sc[t][j] = okc[j] ? __expf(sc[t][j] - m[t]) : 0.f;
        ss += sp[t][j] + sc[t][j];
      }
      sum[t] = ss;
    }
#pragma unroll
    for (int t = 0; t < 4; ++t) sum[t] = 1.0f / xsum16_32(sum[t]);
    f32x4 o[4][2];
#pragma unroll
    for (int t = 0; t < 4; ++t) {
      bf16x8 pp = pack8(sp[t] * sum[t], sc[t] * sum[t]);
#pragma unroll
      for (int et = 0; et < 2; ++et) o[t][et] = mfma16(pack8(av[t][et], av[t + 1][et]), pp, f32x4{0, 0, 0, 0});
    }
#pragma unroll
    for (int t = 0; t < 4; ++t)
#pragma unroll
      for (int et = 0; et < 2; ++et) {
        bf16x4 ob;
#pragma unroll
        for (int j = 0; j < 4; ++j) ob[j] = (__bf16)o[t][et][j];
        bf16* dp = outq + (long)(srow + 16 * t) * DM + h * 28 + 16 * et + 4 * g;
        if (et == 1) dp = (g < 3) ? dp : dummy + lane * 4;
        *(bf16x4*)dp = ob;
      }
  } else {
#pragma unroll
    for (int t = 0; t < 4; ++t)
#pragma unroll
      for (int c = 0; c < 2; ++c) {
        bf16x4 ob;
#pragma unroll
        for (int j = 0; j < 4; ++j) ob[j] = (__bf16)phi_f(bfr(aq[t][c][j]));
        bf16* dp = outq + (long)(srow + 16 * t) * DM + h * 28 + 16 * c + 4 * g;
        if (c == 1) dp = (g < 3) ? dp : dummy + lane * 4;
        *(bf16x4*)dp = ob;
      }
#pragma unroll
    for (int t = 1; t < 5; ++t) {
#pragma unroll
      for (int j = 0; j < 4; ++j) {
        ak[t][0][j] = phi_f(bfr(ak[t][0][j]));
        ak[t][1][j] = phi_f(bfr(ak[t][1][j]));
        if (c16 >= 12) av[t][1][j] = (c16 == 12) ? 1.0f : 0.0f;
      }
    }
    f32x4 kv[2][2];
#pragma unroll
    for (int dc = 0; dc < 2; ++dc)
#pragma unroll
      for (int ec = 0; ec < 2; ++ec) {
        f32x4 a = f32x4{0, 0, 0, 0};
        a = mfma16(pack8(ak[1][dc], ak[2][dc]), pack8(av[1][ec], av[2][ec]), a);
        a = mfma16(pack8(ak[3][dc], ak[4][dc]), pack8(av[3][ec], av[4][ec]), a);
        kv[dc][ec] = a;
      }
    BARRIER();
    f32x4* ex = (f32x4*)(lds) + hh * 256 + lane;
    if (rh == 1) {
#pragma unroll
      for (int i = 0; i < 4; ++i) ex[i * 64] = kv[i >> 1][i & 1];
    }
    BARRIER();
    if (rh == 0) {
      float* pb = part + ((long)rt * 16 + h) * 1024;
#pragma unroll
      for (int i = 0; i < 4; ++i) {
        f32x4 o = kv[i >> 1][i & 1] + ex[i * 64];
        *(f32x4*)(pb + (16 * (i & 1) + c16) * 32 + 16 * (i >> 1) + 4 * g) = o;
      }
    }
  }
}

__global__ __launch_bounds__(256, 1) void k1_qkv(const bf16* __restrict__ xa, const bf16* __restrict__ wa,
                                                 const bf16* __restrict__ xb, const bf16* __restrict__ wb,
                                                 bf16* __restrict__ pq, bf16* __restrict__ ob,
                                                 float* __restrict__ part, bf16* __restrict__ dummy,
                                                 unsigned* __restrict__ flags) {
  extern __shared__ __attribute__((aligned(16))) char lds[];
  const int b = blockIdx.x;
  const int xcd = b & 7, idx = b >> 3;
  const int br = xcd & 1, sub = xcd >> 1;
  const int rt = (sub >> 1) * 8 + (idx >> 2), hg = (sub & 1) * 4 + (idx & 3);
  if (b == 0 && threadIdx.x == 0) flags[0] = 0u;
  if (br == 0) k1_body<0>(xa, wa, pq, part, dummy, rt, hg, lds);
  else         k1_body<1>(xb, wb, ob, part, dummy, rt, hg, lds);
  STAMP(0,5);
}

constexpr int OP_OB = 7 * 64 * 128;
constexpr int OP_WB = 448 * 224;
constexpr int OP_LDS = OP_OB + OP_WB;

DI void op_issue_w(const bf16* __restrict__ wo, int n0, int wv, int lane, unsigned ldsbase) {
  int off[7];
#pragma unroll
  for (int i = 0; i < 7; ++i) {
    int c = i * 64 + lane;
    int lrow = c / 14, pos = c - lrow * 14;
    off[i] = (pi_inv(lrow) * DM + n0 + pos * 8) * 2;
  }
#pragma unroll
  for (int r = 0; r < 4; ++r) {
    int kb = wv + 4 * r;
    if (kb < 14) {
      const char* base = (const char*)wo + (long)kb * 32 * DM * 2;
#pragma unroll
      for (int i = 0; i < 7; ++i) glds16(base + off[i], ldsbase + OP_OB + (kb * 7 + i) * 1024);
    }
  }
}

template <int NC>
DI void op_compute(const char* lds, bf16* __restrict__ out, int m0, int ncol0, int ct0, int rbase, int lane) {
  const int g = lane >> 4, c16 = lane & 15;
  f32x4 acc[2][NC];
#pragma unroll
  for (int r = 0; r < 2; ++r)
#pragma unroll
    for (int c = 0; c < NC; ++c) acc[r][c] = f32x4{0, 0, 0, 0};
  const int ooff = (rbase + c16) * 128;
  const int woff = OP_OB + (4 * g + (c16 >> 2)) * 224 + ct0 * 32 + (c16 & 3) * 8;
#pragma unroll
  for (int ks = 0; ks < 14; ++ks) {
    bf16x8 of[2], wf[NC];
    const int xs = (ks >> 1) * 8192 + ((((ks & 1) * 4 + g) ^ (c16 & 7)) << 4);
#pragma unroll
    for (int r = 0; r < 2; ++r) of[r] = *(const bf16x8*)(lds + ooff + r * 2048 + xs);
#pragma unroll
    for (int c = 0; c < NC; ++c) wf[c] = tr_frag(lds + woff + ks * 32 * 224 + c * 32, 16 * 224);
#pragma unroll
    for (int r = 0; r < 2; ++r)
#pragma unroll
      for (int c = 0; c < NC; ++c) acc[r][c] = mfma16(wf[c], of[r], acc[r][c]);
  }
#pragma unroll
  for (int r = 0; r < 2; ++r)
#pragma unroll
    for (int c = 0; c < NC; ++c) {
      bf16x4 ob;
#pragma unroll
      for (int j = 0; j < 4; ++j) ob[j] = (__bf16)acc[r][c][j];
      *(bf16x4*)(out + (long)(m0 + rbase + r * 16 + c16) * (2 * DM) + ncol0 + (ct0 + c) * 16 + 4 * g) = ob;
    }
}

#define RLX_AGENT __ATOMIC_RELAXED, __HIP_MEMORY_SCOPE_AGENT
typedef __attribute__((address_space(1))) unsigned gu32;
typedef __attribute__((address_space(1))) unsigned long long gu64;

DI bf16x4 reduce_head(const float* __restrict__ part, int h, int tid) {
  const float* p = part + (long)h * 1024 + tid * 4;
  f32x4 v[16];
#pragma unroll
  for (int i = 0; i < 16; ++i) v[i] = *(const f32x4*)(p + (long)i * 16 * 1024);
  f32x4 s = v[0];
#pragma unroll
  for (int i = 1; i < 16; ++i) s += v[i];
  const int e = tid >> 3, d = (tid & 7) * 4;
  bf16x4 r;
#pragma unroll
  for (int j = 0; j < 4; ++j) r[j] = (__bf16)((e <= 28 && d + j < 28) ? s[j] : 0.f);
  return r;
}

__global__ __launch_bounds__(256, 1) void k2_out(const bf16* __restrict__ pq, const bf16* __restrict__ woa,
                                                 const bf16* __restrict__ ob, const bf16* __restrict__ wob,
                                                 const float* __restrict__ part, bf16* kvt,
                                                 unsigned* flags, bf16* slow, bf16* __restrict__ out) {
  extern __shared__ __attribute__((aligned(16))) char lds[];
  const int tid = threadIdx.x, lane = tid & 63;
  const int wv = __builtin_amdgcn_readfirstlane(tid >> 6);
  const int g = lane >> 4, c16 = lane & 15;
  const int b = blockIdx.x;
  const int xcd = b & 7, idx = b >> 3;
  const int br = xcd & 1, sub = xcd >> 1;
  const int mt = sub * 8 + (idx >> 2), nt = idx & 3;
  const int m0 = mt * 64, n0 = nt * 112;
  const unsigned ldsbase = (unsigned)(size_t)lds;
  STAMP(1,0);
  if (br == 1) {
    if (idx < 4) {
      const int h = sub * 4 + idx;
      bf16x4 r = reduce_head(part, h, tid);
      __hip_atomic_store((gu64*)(kvt + h * 1024 + tid * 4), __builtin_bit_cast(unsigned long long, r), RLX_AGENT);
      asm volatile("s_waitcnt vmcnt(0)" ::: "memory");
      __syncthreads();
      if (tid == 0) __hip_atomic_fetch_add((gu32*)flags, 1u, RLX_AGENT);
    }
    op_issue_w(wob, n0, wv, lane, ldsbase);
    {
      const int rsub = lane >> 3, pos = lane & 7;
#pragma unroll
      for (int i = 0; i < 14; ++i) {
        int oi = wv + 4 * i;
        int kc = oi >> 3, row = (oi & 7) * 8 + rsub;
        glds16(ob + (long)(m0 + row) * DM + kc * 64 + ((pos ^ (row & 7)) * 8), ldsbase + oi * 1024);
      }
    }
    STAMP(1,1);
    WAIT_VM(0);
    BARRIER();
    STAMP(1,3);
    if (wv < 2) op_compute<4>(lds, out, m0, DM + n0, 0, (wv & 1) * 32, lane);
    else        op_compute<3>(lds, out, m0, DM + n0, 4, (wv & 1) * 32, lane);
    STAMP(1,4);
    return;
  }
  bf16x8 qf[4][4];
#pragma unroll
  for (int hi = 0; hi < 4; ++hi)
#pragma unroll
    for (int t = 0; t < 4; ++t) {
      const bf16* qp = pq + (long)(m0 + 16 * t + c16) * DM + (wv * 4 + hi) * 28 + 8 * g;
      bf16x4 lo = *(const bf16x4*)qp;
      bf16x4 hi4 = *(const bf16x4*)(qp + 4);
      qf[hi][t] = __builtin_shufflevector(lo, hi4, 0, 1, 2, 3, 4, 5, 6, 7);
    }
  op_issue_w(woa, n0, wv, lane, ldsbase);
  STAMP(1,1);
  volatile LAS int* okw = (volatile LAS int*)(lds + OP_LDS);
  if (wv == 0) {
    int ok = 0;
    for (int spin = 0; spin < 20000; ++spin) {
      if ((unsigned)__builtin_amdgcn_readfirstlane(__hip_atomic_load((gu32*)flags, RLX_AGENT)) == 16u) { ok = 1; break; }
      __builtin_amdgcn_s_sleep(1);
    }
    __builtin_amdgcn_fence(__ATOMIC_ACQUIRE, "agent");
    if (lane == 0) *okw = ok;
  }
  asm volatile("s_waitcnt vmcnt(0)" ::: "memory");
  BARRIER();
  STAMP(1,2);
  const int ok = __builtin_amdgcn_readfirstlane(*okw);
  const bf16* kvs = kvt;
  if (!ok) {
    bf16* mine = slow + (long)b * 16384;
#pragma unroll 1
    for (int h = 0; h < 16; ++h) *(bf16x4*)(mine + h * 1024 + tid * 4) = reduce_head(part, h, tid);
    asm volatile("s_waitcnt vmcnt(0)" ::: "memory");
    __syncthreads();
    kvs = mine;
  }
  bf16x8 kf[4][2];
#pragma unroll
  for (int hi = 0; hi < 4; ++hi) {
    kf[hi][0] = *(const bf16x8*)(kvs + (wv * 4 + hi) * 1024 + c16 * 32 + 8 * g);
    kf[hi][1] = *(const bf16x8*)(kvs + (wv * 4 + hi) * 1024 + (16 + c16) * 32 + 8 * g);
  }
#pragma unroll
  for (int hi = 0; hi < 4; ++hi) {
    const int h = wv * 4 + hi;
    f32x4 n0v[4], n1v[4];
#pragma unroll
    for (int t = 0; t < 4; ++t) {
      bf16x8 q = qf[hi][t];
      if (g == 3) { q[4] = (__bf16)0.f; q[5] = (__bf16)0.f; q[6] = (__bf16)0.f; q[7] = (__bf16)0.f; }
      n0v[t] = mfma16(kf[hi][0], q, f32x4{0, 0, 0, 0});
      n1v[t] = mfma16(kf[hi][1], q, f32x4{0, 0, 0, 0});
    }
#pragma unroll
    for (int t = 0; t < 4; ++t) {
      float den = bcast_row3(n1v[t][0]);
      den = bfr(bfr(den) + 1e-6f);
      const float inv = 1.0f / den;
      bf16x4 o0, o1;
#pragma unroll
      for (int j = 0; j < 4; ++j) {
        o0[j] = (__bf16)(bfr(n0v[t][j]) * inv);
        o1[j] = (__bf16)(bfr(n1v[t][j]) * inv);
      }
      const int row = 16 * t + c16;
      const int col = h * 28 + 4 * g;
      *(bf16x4*)(lds + (col >> 6) * 8192 + row * 128 + ((((col & 63) >> 3) ^ (row & 7)) << 4) + (col & 7) * 2) = o0;
      const int col1 = col + 16;
      int a1 = (col1 >> 6) * 8192 + row * 128 + ((((col1 & 63) >> 3) ^ (row & 7)) << 4) + (col1 & 7) * 2;
      a1 = (g < 3) ? a1 : OP_LDS + 16 + lane * 8;
      *(bf16x4*)(lds + a1) = o1;
    }
  }
  BARRIER();
  STAMP(1,3);
  if (wv < 2) op_compute<4>(lds, out, m0, n0, 0, (wv & 1) * 32, lane);
  else        op_compute<3>(lds, out, m0, n0, 4, (wv & 1) * 32, lane);
  STAMP(1,4);
}

extern "C" void kernel_launch(void* const* d_in, const int* in_sizes, int n_in,
                              void* d_out, int out_size, void* d_ws, size_t ws_size,
                              hipStream_t stream) {
  const bf16* xa = (const bf16*)d_in[0];
  const bf16* wqa = (const bf16*)d_in[1];
  const bf16* woa = (const bf16*)d_in[2];
  const bf16* xb = (const bf16*)d_in[3];
  const bf16* wqb = (const bf16*)d_in[4];
  const bf16* wob = (const bf16*)d_in[5];
  bf16* out = (bf16*)d_out;
  char* ws = (char*)d_ws;
  bf16* pq = (bf16*)ws;
  bf16* ob = (bf16*)(ws + 2 * 1024 * 1024);
  float* part = (float*)(ws + 4 * 1024 * 1024);
  bf16* kvt = (bf16*)(ws + 6 * 1024 * 1024);
  unsigned* flags = (unsigned*)(ws + 7 * 1024 * 1024);
  bf16* dummy = (bf16*)(ws + 7 * 1024 * 1024 + 4096);
  bf16* slow = (bf16*)(ws + 8 * 1024 * 1024);

  (void)hipFuncSetAttribute((const void*)k1_qkv, hipFuncAttributeMaxDynamicSharedMemorySize, K1_LDS);
  (void)hipFuncSetAttribute((const void*)k2_out, hipFuncAttributeMaxDynamicSharedMemorySize, OP_LDS + 1024);

  k1_qkv<<<256, 256, K1_LDS, stream>>>(xa, wqa, xb, wqb, pq, ob, part, dummy, flags);
  k2_out<<<256, 256, OP_LDS + 1024, stream>>>(pq, woa, ob, wob, part, kvt, flags, slow, out);
}
```

```cpp
#include <hip/hip_runtime.h>
#include <hip/hip_bf16.h>
#include <stdint.h>

typedef __bf16 bf16;
typedef __attribute__((ext_vector_type(8))) __bf16 bf16x8;
typedef __attribute__((ext_vector_type(4))) __bf16 bf16x4;
typedef __attribute__((ext_vector_type(4))) float f32x4;
#define LAS __attribute__((address_space(3)))
#define DI __device__ __forceinline__

#define STAMP(k,i) do {} while (0)
constexpr int SEQ = 2048, DM = 448, NQKV = 1344;

DI float bfr(float x) { return (float)(__bf16)x; }

DI void glds16(const void* gsrc, unsigned lds_dst) {
  unsigned keep;
  asm volatile("s_mov_b32 %0, m0\n\ts_mov_b32 m0, %2\n\ts_nop 0\n\tglobal_load_lds_dwordx4 %1, off\n\ts_mov_b32 m0, %0"
               : "=&s"(keep) : "v"(gsrc), "s"(lds_dst) : "memory");
}
#define WAIT_VM(n) asm volatile("s_waitcnt vmcnt(" #n ")" ::: "memory")
#define BARRIER() do { asm volatile("s_waitcnt lgkmcnt(0)" ::: "memory"); __builtin_amdgcn_s_barrier(); asm volatile("" ::: "memory"); } while (0)

DI bf16x8 tr_frag(const char* p, int hi_off) {
  bf16x4 lo = __builtin_amdgcn_ds_read_tr16_b64_v4bf16((LAS bf16x4*)(p));
  bf16x4 hi = __builtin_amdgcn_ds_read_tr16_b64_v4bf16((LAS bf16x4*)(p + hi_off));
  return __builtin_shufflevector(lo, hi, 0, 1, 2, 3, 4, 5, 6, 7);
}
DI f32x4 mfma16(bf16x8 a, bf16x8 b, f32x4 c) { return __builtin_amdgcn_mfma_f32_16x16x32_bf16(a, b, c, 0, 0, 0); }

DI bf16x8 pack8(f32x4 a, f32x4 b) {
  bf16x8 r;
#pragma unroll
  for (int i = 0; i < 4; ++i) { r[i] = (__bf16)a[i]; r[4 + i] = (__bf16)b[i]; }
  return r;
}
DI float phi_f(float t) {
  float r = t > 0.f ? t + 1.0f : bfr(__expf(t)) + 1e-6f;
  return bfr(r);
}
DI float xmax16_32(float m) {
  unsigned u = __float_as_uint(m);
  auto r = __builtin_amdgcn_permlane16_swap(u, u, false, false);
  float a = fmaxf(__uint_as_float(r[0]), __uint_as_float(r[1]));
  unsigned v = __float_as_uint(a);
  auto r2 = __builtin_amdgcn_permlane32_swap(v, v, false, false);
  return fmaxf(__uint_as_float(r2[0]), __uint_as_float(r2[1]));
}
DI float xsum16_32(float m) {
  unsigned u = __float_as_uint(m);
  auto r = __builtin_amdgcn_permlane16_swap(u, u, false, false);
  float a = __uint_as_float(r[0]) + __uint_as_float(r[1]);
  unsigned v = __float_as_uint(a);
  auto r2 = __builtin_amdgcn_permlane32_swap(v, v, false, false);
  return __uint_as_float(r2[0]) + __uint_as_float(r2[1]);
}
DI float bcast_row3(float x) {
  unsigned u = __float_as_uint(x);
  auto r = __builtin_amdgcn_permlane32_swap(u, u, false, false);
  unsigned v = r[1];
  auto r2 = __builtin_amdgcn_permlane16_swap(v, v, false, false);
  return __uint_as_float(r2[1]);
}
DI int pi_inv(int lr) { return lr < 16 ? 8 * (lr >> 2) + (lr & 3) : 8 * ((lr - 16) >> 2) + 4 + (lr & 3); }

constexpr int K1_XB = 144 * 128;
constexpr int K1_WB = 64 * 352;
constexpr int K1_STAGE = K1_XB + K1_WB;
constexpr int K1_LDS = 3 * K1_STAGE;

template <int BR>
DI void k1_body(const bf16* __restrict__ x, const bf16* __restrict__ w, bf16* __restrict__ outq,
                float* __restrict__ part, bf16* __restrict__ dummy, int rt, int hg, char* lds, int wv, int lane) {
  const int rq = wv >> 1, hh = wv & 1;
  const int g = lane >> 4, c16 = lane & 15;
  const int s0 = rt * 128;
  const unsigned ldsbase = (unsigned)(size_t)lds;

  const char* src[5];
  unsigned dst[5];
  long strd2 = 128;
#pragma unroll
  for (int i = 0; i < 5; ++i) {
    const int j = wv + 8 * i;
    if (i < 2 || (i == 2 && j < 18)) {
      int row = j * 8 + (lane >> 3), pos = lane & 7;
      int grow = s0 - 16 + row; grow = grow < 0 ? 0 : grow;
      src[i] = (const char*)(x + (long)grow * DM + ((pos ^ (row & 7)) * 8));
      dst[i] = j * 1024;
    } else {
      int wi = j - 18;
      int c = wi * 64 + lane;
      int lrow = c / 22, pos = c - lrow * 22;
      pos = pos > 20 ? 20 : pos;
      int k = (lrow & 32) + pi_inv(lrow & 31);
      int seg = pos / 7, cc = pos - seg * 7;
      src[i] = (const char*)(w + (long)k * NQKV + seg * DM + hg * 56 + cc * 8);
      dst[i] = K1_XB + wi * 1024;
      if (i == 2) strd2 = 64L * NQKV * 2;
    }
  }
  auto issue = [&](int st, int buf) {
    unsigned b = ldsbase + buf * K1_STAGE;
    glds16(src[0] + st * 128, b + dst[0]);
    glds16(src[1] + st * 128, b + dst[1]);
    glds16(src[2] + st * strd2, b + dst[2]);
    glds16(src[3] + (long)st * (64L * NQKV * 2), b + dst[3]);
    glds16(src[4] + (long)st * (64L * NQKV * 2), b + dst[4]);
  };

  constexpr int T0 = BR ? 0 : 1;
  f32x4 aq[2][2], ak[3][2], av[3][2];
#pragma unroll
  for (int t = 0; t < 3; ++t)
#pragma unroll
    for (int c = 0; c < 2; ++c) {
      if (t < 2) aq[t][c] = f32x4{0, 0, 0, 0};
      ak[t][c] = f32x4{0, 0, 0, 0}; av[t][c] = f32x4{0, 0, 0, 0};
    }

  STAMP(0,0);
  issue(0, 0);
  issue(1, 1);
  const int xoff = (rq * 32 + c16) * 128;
  const int woff = K1_XB + (4 * g + (c16 >> 2)) * 352 + hh * 56 + (c16 & 3) * 8;
  const bool early = wv < 4;
#pragma unroll 1
  for (int st = 0; st < 7; ++st) {
    if (st < 6) WAIT_VM(5); else WAIT_VM(0);
    BARRIER();
    if (st == 0) STAMP(0,2);
    if (st == 3) STAMP(0,3);
    if (early && st + 2 < 7) issue(st + 2, (st + 2) % 3);
    const char* sb = lds + (st % 3) * K1_STAGE;
#pragma unroll
    for (int ks = 0; ks < 2; ++ks) {
      bf16x8 xf[3], wf[6];
      const int xs = ((ks * 4 + g) ^ (c16 & 7)) << 4;
#pragma unroll
      for (int t = T0; t < 3; ++t) xf[t] = *(const bf16x8*)(sb + xoff + t * 2048 + xs);
#pragma unroll
      for (int ct = 0; ct < 6; ++ct)
        wf[ct] = tr_frag(sb + woff + ks * 32 * 352 + (ct >> 1) * 112 + (ct & 1) * 32, 16 * 352);
#pragma unroll
      for (int t = T0; t < 3; ++t) {
#pragma unroll
        for (int c = 0; c < 2; ++c) {
          if (t >= 1) aq[t - 1][c] = mfma16(wf[c], xf[t], aq[t - 1][c]);
          if (BR) ak[t][c] = mfma16(wf[2 + c], xf[t], ak[t][c]);
          else    ak[t][c] = mfma16(xf[t], wf[2 + c], ak[t][c]);
          av[t][c] = mfma16(xf[t], wf[4 + c], av[t][c]);
        }
      }
    }
    if (!early && st + 2 < 7) issue(st + 2, (st + 2) % 3);
  }

  STAMP(0,4);
  const int h = hg * 2 + hh;
  const int srow = s0 + rq * 32 + c16;
  if (BR) {
    const float scale = 0.18898223650461362f;
    bf16x8 kp[3], qp[2];
#pragma unroll
    for (int t = 0; t < 3; ++t) {
      f32x4 hi = ak[t][1];
      if (g == 3) hi = f32x4{0, 0, 0, 0};
      kp[t] = pack8(ak[t][0], hi);
    }
#pragma unroll
    for (int t = 0; t < 2; ++t) {
      f32x4 hi = aq[t][1];
      if (g == 3) hi = f32x4{0, 0, 0, 0};
      qp[t] = pack8(aq[t][0], hi);
    }
    f32x4 sp[2], sc[2];
#pragma unroll
    for (int t = 0; t < 2; ++t) {
      sp[t] = mfma16(kp[t], qp[t], f32x4{0, 0, 0, 0});
      sc[t] = mfma16(kp[t + 1], qp[t], f32x4{0, 0, 0, 0});
    }
    bool okp[4], okc[4];
#pragma unroll
    for (int j = 0; j < 4; ++j) {
      int dp = c16 + 16 - 4 * g - j, dc = c16 - 4 * g - j;
      okp[j] = ((dp & 1) == 0) && dp <= 14;
      okc[j] = dc >= 0 && ((dc & 1) == 0);
    }
    const bool first = (s0 + rq * 32) == 0;
    float m[2];
#pragma unroll
    for (int t = 0; t < 2; ++t) {
      float mm = -3.0e38f;
#pragma unroll
      for (int j = 0; j < 4; ++j) {
        float lp = bfr(bfr(sp[t][j]) * scale), lc = bfr(bfr(sc[t][j]) * scale);
        bool op = okp[j] && !(t == 0 && first);
        sp[t][j] = op ? lp : -3.0e38f;
        sc[t][j] = okc[j] ? lc : -3.0e38f;
        mm = fmaxf(mm, fmaxf(sp[t][j], sc[t][j]));
      }
      m[t] = mm;
    }
#pragma unroll
    for (int t = 0; t < 2; ++t) m[t] = xmax16_32(m[t]);
    float sum[2];
#pragma unroll
    for (int t = 0; t < 2; ++t) {
      float ss = 0.f;
#pragma unroll
      for (int j = 0; j < 4; ++j) {
        bool op = okp[j] && !(t == 0 && first);
        sp[t][j] = op ? __expf(sp[t][j] - m[t]) : 0.f;
        sc[t][j] = okc[j] ? __expf(sc[t][j] - m[t]) : 0.f;
        ss += sp[t][j] + sc[t][j];
      }
      sum[t] = ss;
    }
#pragma unroll
    for (int t = 0; t < 2; ++t) sum[t] = 1.0f / xsum16_32(sum[t]);
    f32x4 o[2][2];
#pragma unroll
    for (int t = 0; t < 2; ++t) {
      bf16x8 pp = pack8(sp[t] * sum[t], sc[t] * sum[t]);
#pragma unroll
      for (int et = 0; et < 2; ++et) o[t][et] = mfma16(pack8(av[t][et], av[t + 1][et]), pp, f32x4{0, 0, 0, 0});
    }
#pragma unroll
    for (int t = 0; t < 2; ++t)
#pragma unroll
      for (int et = 0; et < 2; ++et) {
        bf16x4 ob;
#pragma unroll
        for (int j = 0; j < 4; ++j) ob[j] = (__bf16)o[t][et][j];
        bf16* dp = outq + (long)(srow + 16 * t) * DM + h * 28 + 16 * et + 4 * g;
        if (et == 1) dp = (g < 3) ? dp : dummy + lane * 4;
        *(bf16x4*)dp = ob;
      }
  } else {
#pragma unroll
    for (int t = 0; t < 2; ++t)
#pragma unroll
      for (int c = 0; c < 2; ++c) {
        bf16x4 ob;
#pragma unroll
        for (int j = 0; j < 4; ++j) ob[j] = (__bf16)phi_f(bfr(aq[t][c][j]));
        bf16* dp = outq + (long)(srow + 16 * t) * DM + h * 28 + 16 * c + 4 * g;
        if (c == 1) dp = (g < 3) ? dp : dummy + lane * 4;
        *(bf16x4*)dp = ob;
      }
#pragma unroll
    for (int t = 1; t < 3; ++t) {
#pragma unroll
      for (int j = 0; j < 4; ++j) {
        ak[t][0][j] = phi_f(bfr(ak[t][0][j]));
        ak[t][1][j] = phi_f(bfr(ak[t][1][j]));
        if (c16 >= 12) av[t][1][j] = (c16 == 12) ? 1.0f : 0.0f;
      }
    }
    f32x4 kv[2][2];
#pragma unroll
    for (int dc = 0; dc < 2; ++dc)
#pragma unroll
      for (int ec = 0; ec < 2; ++ec)
        kv[dc][ec] = mfma16(pack8(ak[1][dc], ak[2][dc]), pack8(av[1][ec], av[2][ec]), f32x4{0, 0, 0, 0});
    BARRIER();
    f32x4* ex = (f32x4*)(lds) + hh * 1024 + lane;
    if (rq != 0) {
#pragma unroll
      for (int i = 0; i < 4; ++i) ex[(rq * 4 + i) * 64] = kv[i >> 1][i & 1];
    }
    BARRIER();
    if (rq == 0) {
      float* pb = part + ((long)rt * 16 + h) * 1024;
#pragma unroll
      for (int i = 0; i < 4; ++i) {
        f32x4 o = ((kv[i >> 1][i & 1] + ex[(4 + i) * 64]) + ex[(8 + i) * 64]) + ex[(12 + i) * 64];
        *(f32x4*)(pb + (16 * (i & 1) + c16) * 32 + 16 * (i >> 1) + 4 * g) = o;
      }
    }
  }
}

__global__ __launch_bounds__(512, 2) void k1_qkv(const bf16* __restrict__ xa, const bf16* __restrict__ wa,
                                                 const bf16* __restrict__ xb, const bf16* __restrict__ wb,
                                                 bf16* __restrict__ pq, bf16* __restrict__ ob,
                                                 float* __restrict__ part, bf16* __restrict__ dummy,
                                                 unsigned* __restrict__ flags) {
  extern __shared__ __attribute__((aligned(16))) char lds[];
  const int b = blockIdx.x;
  const int lane = threadIdx.x & 63;
  const int wv = __builtin_amdgcn_readfirstlane(threadIdx.x >> 6);
  const int xcd = b & 7, idx = b >> 3;
  const int br = xcd & 1, sub = xcd >> 1;
  const int rt = (sub >> 1) * 8 + (idx >> 2), hg = (sub & 1) * 4 + (idx & 3);
  if (b == 0 && threadIdx.x == 0) flags[0] = 0u;
  if (br == 0) k1_body<0>(xa, wa, pq, part, dummy, rt, hg, lds, wv, lane);
  else         k1_body<1>(xb, wb, ob, part, dummy, rt, hg, lds, wv, lane);
  STAMP(0,5);
}

constexpr int OP_OB = 7 * 64 * 128;
constexpr int OP_WB = 448 * 224;
constexpr int OP_LDS = OP_OB + OP_WB;

template <int NW>
DI void op_issue(const bf16* __restrict__ a, const bf16* __restrict__ wo, int m0, int n0, int wv, int lane,
                 unsigned ldsbase, bool branch_a) {
  int woffs[NW];
#pragma unroll
  for (int i = 0; i < NW; ++i) {
    int c = (wv + 8 * i) * 64 + lane;
    int lrow = c / 14, pos = c - lrow * 14;
    woffs[i] = (((lrow & 32) + pi_inv(lrow & 31)) * DM + n0 + pos * 8) * 2;
  }
  const int row = wv * 8 + (lane >> 3), pos = lane & 7;
  const int aoff = ((m0 + row) * DM + ((pos ^ (row & 7)) * 8)) * 2;
  if (branch_a) {
#pragma unroll
    for (int kc = 0; kc < 7; ++kc) glds16((const char*)a + aoff + kc * 128, ldsbase + kc * 8192 + wv * 1024);
#pragma unroll
    for (int kc = 0; kc < 7; ++kc)
#pragma unroll
      for (int i = 0; i < NW; ++i)
        glds16((const char*)wo + woffs[i] + kc * (64 * DM * 2), ldsbase + OP_OB + kc * 14336 + (wv + 8 * i) * 1024);
  } else {
#pragma unroll
    for (int kc = 0; kc < 7; ++kc) {
      glds16((const char*)a + aoff + kc * 128, ldsbase + kc * 8192 + wv * 1024);
#pragma unroll
      for (int i = 0; i < NW; ++i)
        glds16((const char*)wo + woffs[i] + kc * (64 * DM * 2), ldsbase + OP_OB + kc * 14336 + (wv + 8 * i) * 1024);
    }
  }
}
template <int NPC> DI void chunk_wait(int kc) {
  switch (kc) {
    case 0: if (NPC == 3) WAIT_VM(18); else WAIT_VM(12); break;
    case 1: if (NPC == 3) WAIT_VM(15); else WAIT_VM(10); break;
    case 2: if (NPC == 3) WAIT_VM(12); else WAIT_VM(8); break;
    case 3: if (NPC == 3) WAIT_VM(9); else WAIT_VM(6); break;
    case 4: if (NPC == 3) WAIT_VM(6); else WAIT_VM(4); break;
    case 5: if (NPC == 3) WAIT_VM(3); else WAIT_VM(2); break;
    default: WAIT_VM(0); break;
  }
}

template <int NC, bool CHUNK_BARRIERS>
DI void op_compute(const char* lds, bf16* __restrict__ out, int m0, int ncol0, int ct0, int rbase, int lane) {
  const int g = lane >> 4, c16 = lane & 15;
  f32x4 acc[2][NC];
#pragma unroll
  for (int r = 0; r < 2; ++r)
#pragma unroll
    for (int c = 0; c < NC; ++c) acc[r][c] = f32x4{0, 0, 0, 0};
  const int ooff = (rbase + c16) * 128;
  const int woff = OP_OB + (4 * g + (c16 >> 2)) * 224 + ct0 * 32 + (c16 & 3) * 8;
#pragma unroll
  for (int ks = 0; ks < 14; ++ks) {
    if (CHUNK_BARRIERS && (ks & 1) == 0) { chunk_wait<3>(ks >> 1); BARRIER(); }
    bf16x8 of[2], wf[NC];
    const int xs = (ks >> 1) * 8192 + ((((ks & 1) * 4 + g) ^ (c16 & 7)) << 4);
#pragma unroll
    for (int r = 0; r < 2; ++r) of[r] = *(const bf16x8*)(lds + ooff + r * 2048 + xs);
#pragma unroll
    for (int c = 0; c < NC; ++c) wf[c] = tr_frag(lds + woff + ks * 32 * 224 + c * 32, 16 * 224);
#pragma unroll
    for (int r = 0; r < 2; ++r)
#pragma unroll
      for (int c = 0; c < NC; ++c) acc[r][c] = mfma16(wf[c], of[r], acc[r][c]);
  }
#pragma unroll
  for (int r = 0; r < 2; ++r)
#pragma unroll
    for (int c = 0; c < NC; ++c) {
      bf16x4 ob;
#pragma unroll
      for (int j = 0; j < 4; ++j) ob[j] = (__bf16)acc[r][c][j];
      *(bf16x4*)(out + (long)(m0 + rbase + r * 16 + c16) * (2 * DM) + ncol0 + (ct0 + c) * 16 + 4 * g) = ob;
    }
}

#define RLX_AGENT __ATOMIC_RELAXED, __HIP_MEMORY_SCOPE_AGENT
typedef __attribute__((address_space(1))) unsigned gu32;
typedef __attribute__((address_space(1))) unsigned long long gu64;

DI bf16x4 reduce_head(const float* __restrict__ part, int h, int tid) {
  const float* p = part + (long)h * 1024 + tid * 4;
  f32x4 v[16];
#pragma unroll
  for (int i = 0; i < 16; ++i) v[i] = *(const f32x4*)(p + (long)i * 16 * 1024);
  f32x4 s = v[0];
#pragma unroll
  for (int i = 1; i < 16; ++i) s += v[i];
  const int e = tid >> 3, d = (tid & 7) * 4;
  bf16x4 r;
#pragma unroll
  for (int j = 0; j < 4; ++j) r[j] = (__bf16)((e <= 28 && d + j < 28) ? s[j] : 0.f);
  return r;
}

__global__ __launch_bounds__(512, 2) void k2_out(const bf16* __restrict__ pq, const bf16* __restrict__ woa,
                                                 const bf16* __restrict__ ob, const bf16* __restrict__ wob,
                                                 const float* __restrict__ part, bf16* kvt,
                                                 unsigned* flags, bf16* slow, bf16* __restrict__ out) {
  extern __shared__ __attribute__((aligned(16))) char lds[];
  const int tid = threadIdx.x, lane = tid & 63;
  const int wv = __builtin_amdgcn_readfirstlane(tid >> 6);
  const int g = lane >> 4, c16 = lane & 15;
  const int b = blockIdx.x;
  const int xcd = b & 7, idx = b >> 3;
  const int br = xcd & 1, sub = xcd >> 1;
  const int mt = sub * 8 + (idx >> 2), nt = idx & 3;
  const int m0 = mt * 64, n0 = nt * 112;
  const unsigned ldsbase = (unsigned)(size_t)lds;
  STAMP(1,0);
  if (br == 1) {
    if (idx < 4 && wv < 4) {
      const int h = sub * 4 + idx;
      bf16x4 r = reduce_head(part, h, tid);
      __hip_atomic_store((gu64*)(kvt + h * 1024 + tid * 4), __builtin_bit_cast(unsigned long long, r), RLX_AGENT);
      asm volatile("s_waitcnt vmcnt(0)" ::: "memory");
      if (lane == 0) __hip_atomic_fetch_add((gu32*)flags, 1u, RLX_AGENT);
    }
    if (wv < 6) op_issue<2>(ob, wob, m0, n0, wv, lane, ldsbase, false);
    else        op_issue<1>(ob, wob, m0, n0, wv, lane, ldsbase, false);
    STAMP(1,1);
    if (wv < 2)      op_compute<4, true>(lds, out, m0, DM + n0, 0, (wv & 1) * 32, lane);
    else if (wv < 4) op_compute<3, true>(lds, out, m0, DM + n0, 4, (wv & 1) * 32, lane);
    else if (wv < 6) {
#pragma unroll
      for (int kc = 0; kc < 7; ++kc) { chunk_wait<3>(kc); __builtin_amdgcn_s_barrier(); }
    } else {
#pragma unroll
      for (int kc = 0; kc < 7; ++kc) { chunk_wait<2>(kc); __builtin_amdgcn_s_barrier(); }
    }
    STAMP(1,4);
    return;
  }
  if (wv < 6) { op_issue<2>(pq, woa, m0, n0, wv, lane, ldsbase, true); WAIT_VM(14); }
  else        { op_issue<1>(pq, woa, m0, n0, wv, lane, ldsbase, true); WAIT_VM(7); }
  BARRIER();
  STAMP(1,1);
  bf16x8 qf[2][4];
#pragma unroll
  for (int hi = 0; hi < 2; ++hi)
#pragma unroll
    for (int t = 0; t < 4; ++t) {
      const int row = 16 * t + c16;
      const int col = (wv * 2 + hi) * 28 + 8 * g, col1 = col + 4;
      bf16x4 lo = *(const bf16x4*)(lds + (col >> 6) * 8192 + row * 128 + ((((col & 63) >> 3) ^ (row & 7)) << 4) + (col & 7) * 2);
      int a1 = (col1 >> 6) * 8192 + row * 128 + ((((col1 & 63) >> 3) ^ (row & 7)) << 4) + (col1 & 7) * 2;
      bf16x4 hi4 = *(const bf16x4*)(lds + a1);
      if (g == 3) { hi4[0] = (__bf16)0.f; hi4[1] = (__bf16)0.f; hi4[2] = (__bf16)0.f; hi4[3] = (__bf16)0.f; }
      qf[hi][t] = __builtin_shufflevector(lo, hi4, 0, 1, 2, 3, 4, 5, 6, 7);
    }
  volatile LAS int* okw = (volatile LAS int*)(lds + OP_LDS);
  if (wv == 0) {
    int ok = 0;
    for (int spin = 0; spin < 20000; ++spin) {
      if ((unsigned)__builtin_amdgcn_readfirstlane(__hip_atomic_load((gu32*)flags, RLX_AGENT)) == 64u) { ok = 1; break; }
      __builtin_amdgcn_s_sleep(1);
    }
    __builtin_amdgcn_fence(__ATOMIC_ACQUIRE, "agent");
    if (lane == 0) *okw = ok;
  }
  asm volatile("s_waitcnt vmcnt(0)" ::: "memory");
  BARRIER();
  STAMP(1,2);
  const int ok = __builtin_amdgcn_readfirstlane(*okw);
  const bf16* kvs = kvt;
  if (!ok) {
    bf16* mine = slow + (long)b * 16384;
#pragma unroll 1
    for (int i = 0; i < 8; ++i) {
      const int h = wv * 2 + (i >> 2), t4 = (i & 3) * 64 + lane;
      *(bf16x4*)(mine + h * 1024 + t4 * 4) = reduce_head(part, h, t4);
    }
    asm volatile("s_waitcnt vmcnt(0)" ::: "memory");
    kvs = mine;
  }
  bf16x8 kf[2][2];
#pragma unroll
  for (int hi = 0; hi < 2; ++hi) {
    kf[hi][0] = *(const bf16x8*)(kvs + (wv * 2 + hi) * 1024 + c16 * 32 + 8 * g);
    kf[hi][1] = *(const bf16x8*)(kvs + (wv * 2 + hi) * 1024 + (16 + c16) * 32 + 8 * g);
  }
#pragma unroll
  for (int hi = 0; hi < 2; ++hi) {
    const int h = wv * 2 + hi;
    f32x4 n0v[4], n1v[4];
#pragma unroll
    for (int t = 0; t < 4; ++t) {
      n0v[t] = mfma16(kf[hi][0], qf[hi][t], f32x4{0, 0, 0, 0});
      n1v[t] = mfma16(kf[hi][1], qf[hi][t], f32x4{0, 0, 0, 0});
    }
#pragma unroll
    for (int t = 0; t < 4; ++t) {
      float den = bcast_row3(n1v[t][0]);
      den = bfr(bfr(den) + 1e-6f);
      const float inv = 1.0f / den;
      bf16x4 o0, o1;
#pragma unroll
      for (int j = 0; j < 4; ++j) {
        o0[j] = (__bf16)(bfr(n0v[t][j]) * inv);
        o1[j] = (__bf16)(bfr(n1v[t][j]) * inv);
      }
      const int row = 16 * t + c16;
      const int col = h * 28 + 4 * g;
      *(bf16x4*)(lds + (col >> 6) * 8192 + row * 128 + ((((col & 63) >> 3) ^ (row & 7)) << 4) + (col & 7) * 2) = o0;
      const int col1 = col + 16;
      int a1 = (col1 >> 6) * 8192 + row * 128 + ((((col1 & 63) >> 3) ^ (row & 7)) << 4) + (col1 & 7) * 2;
      a1 = (g < 3) ? a1 : OP_LDS + 16 + lane * 8;
      *(bf16x4*)(lds + a1) = o1;
    }
  }
  BARRIER();
  STAMP(1,3);
  if (wv < 2)      op_compute<4, false>(lds, out, m0, n0, 0, (wv & 1) * 32, lane);
  else if (wv < 4) op_compute<3, false>(lds, out, m0, n0, 4, (wv & 1) * 32, lane);
  STAMP(1,4);
}

extern "C" void kernel_launch(void* const* d_in, const int* in_sizes, int n_in,
                              void* d_out, int out_size, void* d_ws, size_t ws_size,
                              hipStream_t stream) {
  const bf16* xa = (const bf16*)d_in[0];
  const bf16* wqa = (const bf16*)d_in[1];
  const bf16* woa = (const bf16*)d_in[2];
  const bf16* xb = (const bf16*)d_in[3];
  const bf16* wqb = (const bf16*)d_in[4];
  const bf16* wob = (const bf16*)d_in[5];
  bf16* out = (bf16*)d_out;
  char* ws = (char*)d_ws;
  bf16* pq = (bf16*)ws;
  bf16* ob = (bf16*)(ws + 2 * 1024 * 1024);
  float* part = (float*)(ws + 4 * 1024 * 1024);
  bf16* kvt = (bf16*)(ws + 6 * 1024 * 1024);
  unsigned* flags = (unsigned*)(ws + 7 * 1024 * 1024);
  bf16* dummy = (bf16*)(ws + 7 * 1024 * 1024 + 4096);
  bf16* slow = (bf16*)(ws + 8 * 1024 * 1024);

  (void)hipFuncSetAttribute((const void*)k1_qkv, hipFuncAttributeMaxDynamicSharedMemorySize, K1_LDS);
  (void)hipFuncSetAttribute((const void*)k2_out, hipFuncAttributeMaxDynamicSharedMemorySize, OP_LDS + 1024);

  k1_qkv<<<256, 512, K1_LDS, stream>>>(xa, wqa, xb, wqb, pq, ob, part, dummy, flags);
  k2_out<<<256, 512, OP_LDS + 1024, stream>>>(pq, woa, ob, wob, part, kvt, flags, slow, out);
}
```

```cpp
#include <hip/hip_runtime.h>
#include <hip/hip_bf16.h>
#include <stdint.h>

typedef __bf16 bf16;
typedef __attribute__((ext_vector_type(8))) __bf16 bf16x8;
typedef __attribute__((ext_vector_type(4))) __bf16 bf16x4;
typedef __attribute__((ext_vector_type(4))) float f32x4;
#define LAS __attribute__((address_space(3)))
#define DI __device__ __forceinline__

#define STAMP(k,i) do {} while (0)
constexpr int SEQ = 2048, DM = 448, NQKV = 1344;

DI float bfr(float x) { return (float)(__bf16)x; }

typedef __attribute__((ext_vector_type(2))) __bf16 bf16x2;
DI f32x4 bfr4(f32x4 v) {
  bf16x2 a = {(__bf16)v[0], (__bf16)v[1]}, b = {(__bf16)v[2], (__bf16)v[3]};
  unsigned ua = __builtin_bit_cast(unsigned, a), ub = __builtin_bit_cast(unsigned, b);
  return f32x4{__uint_as_float(ua << 16), __uint_as_float(ua & 0xffff0000u), __uint_as_float(ub << 16), __uint_as_float(ub & 0xffff0000u)};
}
DI void glds16(const void* gsrc, unsigned lds_dst) {
  unsigned keep;
  asm volatile("s_mov_b32 %0, m0\n\ts_mov_b32 m0, %2\n\ts_nop 0\n\tglobal_load_lds_dwordx4 %1, off\n\ts_mov_b32 m0, %0"
               : "=&s"(keep) : "v"(gsrc), "s"(lds_dst) : "memory");
}
#define WAIT_VM(n) asm volatile("s_waitcnt vmcnt(" #n ")" ::: "memory")
#define BARRIER() do { asm volatile("s_waitcnt lgkmcnt(0)" ::: "memory"); __builtin_amdgcn_s_barrier(); asm volatile("" ::: "memory"); } while (0)

DI bf16x8 tr_frag(const char* p, int hi_off) {
  bf16x4 lo = __builtin_amdgcn_ds_read_tr16_b64_v4bf16((LAS bf16x4*)(p));
  bf16x4 hi = __builtin_amdgcn_ds_read_tr16_b64_v4bf16((LAS bf16x4*)(p + hi_off));
  return __builtin_shufflevector(lo, hi, 0, 1, 2, 3, 4, 5, 6, 7);
}
DI f32x4 mfma16(bf16x8 a, bf16x8 b, f32x4 c) { return __builtin_amdgcn_mfma_f32_16x16x32_bf16(a, b, c, 0, 0, 0); }

DI bf16x8 pack8(f32x4 a, f32x4 b) {
  bf16x8 r;
#pragma unroll
  for (int i = 0; i < 4; ++i) { r[i] = (__bf16)a[i]; r[4 + i] = (__bf16)b[i]; }
  return r;
}
DI float phi_f(float t) {
  float r = t > 0.f ? t + 1.0f : bfr(__expf(t)) + 1e-6f;
  return bfr(r);
}
DI f32x4 phi4(f32x4 acc) {
  f32x4 t = bfr4(acc), e;
#pragma unroll
  for (int j = 0; j < 4; ++j) e[j] = __expf(t[j]);
  e = bfr4(e);
#pragma unroll
  for (int j = 0; j < 4; ++j) e[j] = t[j] > 0.f ? t[j] + 1.0f : e[j] + 1e-6f;
  return e;
}
DI float xmax16_32(float m) {
  unsigned u = __float_as_uint(m);
  auto r = __builtin_amdgcn_permlane16_swap(u, u, false, false);
  float a = fmaxf(__uint_as_float(r[0]), __uint_as_float(r[1]));
  unsigned v = __float_as_uint(a);
  auto r2 = __builtin_amdgcn_permlane32_swap(v, v, false, false);
  return fmaxf(__uint_as_float(r2[0]), __uint_as_float(r2[1]));
}
DI float xsum16_32(float m) {
  unsigned u = __float_as_uint(m);
  auto r = __builtin_amdgcn_permlane16_swap(u, u, false, false);
  float a = __uint_as_float(r[0]) + __uint_as_float(r[1]);
  unsigned v = __float_as_uint(a);
  auto r2 = __builtin_amdgcn_permlane32_swap(v, v, false, false);
  return __uint_as_float(r2[0]) + __uint_as_float(r2[1]);
}
DI float bcast_row3(float x) {
  unsigned u = __float_as_uint(x);
  auto r = __builtin_amdgcn_permlane32_swap(u, u, false, false);
  unsigned v = r[1];
  auto r2 = __builtin_amdgcn_permlane16_swap(v, v, false, false);
  return __uint_as_float(r2[1]);
}
template <int NM, int ND> DI void interleave() {
  constexpr int LEAD = ND > NM ? ND - NM : 0;
  if (LEAD) __builtin_amdgcn_sched_group_barrier(0x100, LEAD, 0);
#pragma unroll
  for (int i = 0; i < NM; ++i) {
    __builtin_amdgcn_sched_group_barrier(0x008, 1, 0);
    if (i + LEAD < ND) __builtin_amdgcn_sched_group_barrier(0x100, 1, 0);
  }
}
DI int pi_inv(int lr) { return lr < 16 ? 8 * (lr >> 2) + (lr & 3) : 8 * ((lr - 16) >> 2) + 4 + (lr & 3); }

constexpr int K1_XB = 144 * 128;
constexpr int K1_WB = 64 * 352;
constexpr int K1_STAGE = K1_XB + K1_WB;
constexpr int K1_LDS = 3 * K1_STAGE;

DI void k1_loader(const bf16* __restrict__ x, const bf16* __restrict__ w, int rt, int hg, char* lds, int wv, int lane) {
  const int s0 = rt * 128;
  const unsigned ldsbase = (unsigned)(size_t)lds;
  const char* src[10];
  unsigned dst[10];
  long strd0;
#pragma unroll
  for (int i = 0; i < 4; ++i) {
    int xi = 2 + wv + 4 * i;
    int row = xi * 8 + (lane >> 3), pos = lane & 7;
    int grow = s0 - 16 + row;
    src[i] = (const char*)(x + (long)grow * DM + ((pos ^ (row & 7)) * 8));
    dst[i] = xi * 1024;
  }
#pragma unroll
  for (int i = 0; i < 6; ++i) {
    int j = wv + 4 * i;
    if (i == 0 && j < 2) {
      int row = j * 8 + (lane >> 3), pos = lane & 7;
      int grow = s0 - 16 + row; grow = grow < 0 ? 0 : grow;
      src[4] = (const char*)(x + (long)grow * DM + ((pos ^ (row & 7)) * 8));
      dst[4] = j * 1024;
      strd0 = 128;
    } else {
      int wi = j - 2;
      int c = wi * 64 + lane;
      int lrow = c / 22, pos = c - lrow * 22;
      pos = pos > 20 ? 20 : pos;
      int k = (lrow & 32) + pi_inv(lrow & 31);
      int seg = pos / 7, cc = pos - seg * 7;
      src[4 + i] = (const char*)(w + (long)k * NQKV + seg * DM + hg * 56 + cc * 8);
      dst[4 + i] = K1_XB + wi * 1024;
      if (i == 0) strd0 = 64L * NQKV * 2;
    }
  }
  auto issue = [&](int st, int buf) {
    unsigned b = ldsbase + buf * K1_STAGE;
#pragma unroll
    for (int i = 0; i < 4; ++i) glds16(src[i] + st * 128, b + dst[i]);
    glds16(src[4] + st * strd0, b + dst[4]);
#pragma unroll
    for (int i = 5; i < 10; ++i) glds16(src[i] + (long)st * (64L * NQKV * 2), b + dst[i]);
  };
  issue(0, 0);
  issue(1, 1);
#pragma unroll 1
  for (int st = 0; st < 7; ++st) {
    if (st < 6) WAIT_VM(10); else WAIT_VM(0);
    __builtin_amdgcn_s_barrier();
    if (st + 2 < 7) issue(st + 2, (st + 2) % 3);
  }
}

template <int BR>
DI void k1_body(bf16* __restrict__ outq, float* __restrict__ part, bf16* __restrict__ dummy, int rt, int hg, char* lds,
                int wv, int lane) {
  const int rq = wv >> 1, hh = wv & 1;
  const int g = lane >> 4, c16 = lane & 15;
  const int s0 = rt * 128;

  constexpr int T0 = BR ? 0 : 1;
  f32x4 aq[2][2], ak[3][2], av[3][2];
#pragma unroll
  for (int t = 0; t < 3; ++t)
#pragma unroll
    for (int c = 0; c < 2; ++c) {
      if (t < 2) aq[t][c] = f32x4{0, 0, 0, 0};
      ak[t][c] = f32x4{0, 0, 0, 0}; av[t][c] = f32x4{0, 0, 0, 0};
    }

  STAMP(0,0);
  const int xoff = (rq * 32 + c16) * 128;
  const int woff = K1_XB + (4 * g + (c16 >> 2)) * 352 + hh * 56 + (c16 & 3) * 8;
  struct Frags { bf16x8 xf[3], wf[6]; };
  auto ldf = [&](Frags& f, const char* sb, int ks) {
    const int xs = ((ks * 4 + g) ^ (c16 & 7)) << 4;
#pragma unroll
    for (int ct = 0; ct < 6; ++ct)
      f.wf[ct] = tr_frag(sb + woff + ks * 32 * 352 + (ct >> 1) * 112 + (ct & 1) * 32, 16 * 352);
#pragma unroll
    for (int t = T0; t < 3; ++t) f.xf[t] = *(const bf16x8*)(sb + xoff + t * 2048 + xs);
  };
  auto mma = [&](const Frags& f) {
#pragma unroll
    for (int t = T0; t < 3; ++t) {
#pragma unroll
      for (int c = 0; c < 2; ++c) {
        if (t >= 1) aq[t - 1][c] = mfma16(f.wf[c], f.xf[t], aq[t - 1][c]);
        if (BR) ak[t][c] = mfma16(f.wf[2 + c], f.xf[t], ak[t][c]);
        else    ak[t][c] = mfma16(f.xf[t], f.wf[2 + c], ak[t][c]);
        av[t][c] = mfma16(f.xf[t], f.wf[4 + c], av[t][c]);
      }
    }
  };
  Frags fa, fb;
  BARRIER();
  STAMP(0,2);
  ldf(fa, lds, 0);
  int buf = 0;
#pragma unroll 1
  for (int st = 0; st < 6; ++st) {
    if (st == 3) STAMP(0,3);
    ldf(fb, lds + buf * K1_STAGE, 1);
    mma(fa);
    interleave<BR ? 16 : 12, BR ? 15 : 14>();
    __builtin_amdgcn_sched_barrier(0);
    buf = buf == 2 ? 0 : buf + 1;
    BARRIER();
    ldf(fa, lds + buf * K1_STAGE, 0);
    mma(fb);
    interleave<BR ? 16 : 12, BR ? 15 : 14>();
    __builtin_amdgcn_sched_barrier(0);
  }
  ldf(fb, lds + buf * K1_STAGE, 1);
  mma(fa);
  interleave<BR ? 16 : 12, BR ? 15 : 14>();
  __builtin_amdgcn_sched_barrier(0);
  mma(fb);

  STAMP(0,4);
  const int h = hg * 2 + hh;
  const int srow = s0 + rq * 32 + c16;
  if (BR) {
    const float scale = 0.18898223650461362f;
    bf16x8 kp[3], qp[2];
#pragma unroll
    for (int t = 0; t < 3; ++t) {
      f32x4 hi = ak[t][1];
      if (g == 3) hi = f32x4{0, 0, 0, 0};
      kp[t] = pack8(ak[t][0], hi);
    }
#pragma unroll
    for (int t = 0; t < 2; ++t) {
      f32x4 hi = aq[t][1];
      if (g == 3) hi = f32x4{0, 0, 0, 0};
      qp[t] = pack8(aq[t][0], hi);
    }
    f32x4 sp[2], sc[2];
#pragma unroll
    for (int t = 0; t < 2; ++t) {
      sp[t] = mfma16(kp[t], qp[t], f32x4{0, 0, 0, 0});
      sc[t] = mfma16(kp[t + 1], qp[t], f32x4{0, 0, 0, 0});
    }
    bool okp[4], okc[4];
#pragma unroll
    for (int j = 0; j < 4; ++j) {
      int dp = c16 + 16 - 4 * g - j, dc = c16 - 4 * g - j;
      okp[j] = ((dp & 1) == 0) && dp <= 14;
      okc[j] = dc >= 0 && ((dc & 1) == 0);
    }
    const bool first = (s0 + rq * 32) == 0;
    float m[2];
#pragma unroll
    for (int t = 0; t < 2; ++t) {
      float mm = -3.0e38f;
      f32x4 lp = bfr4(bfr4(sp[t]) * scale), lc = bfr4(bfr4(sc[t]) * scale);
#pragma unroll
      for (int j = 0; j < 4; ++j) {
        bool op = okp[j] && !(t == 0 && first);
        sp[t][j] = op ? lp[j] : -3.0e38f;
        sc[t][j] = okc[j] ? lc[j] : -3.0e38f;
        mm = fmaxf(mm, fmaxf(sp[t][j], sc[t][j]));
      }
      m[t] = mm;
    }
#pragma unroll
    for (int t = 0; t < 2; ++t) m[t] = xmax16_32(m[t]);
    float sum[2];
#pragma unroll
    for (int t = 0; t < 2; ++t) {
      float ss = 0.f;
#pragma unroll
      for (int j = 0; j < 4; ++j) {
        bool op = okp[j] && !(t == 0 && first);
        sp[t][j] = op ? __expf(sp[t][j] - m[t]) : 0.f;
        sc[t][j] = okc[j] ? __expf(sc[t][j] - m[t]) : 0.f;
        ss += sp[t][j] + sc[t][j];
      }
      sum[t] = ss;
    }
#pragma unroll
    for (int t = 0; t < 2; ++t) sum[t] = 1.0f / xsum16_32(sum[t]);
    f32x4 o[2][2];
#pragma unroll
    for (int t = 0; t < 2; ++t) {
      bf16x8 pp = pack8(sp[t] * sum[t], sc[t] * sum[t]);
#pragma unroll
      for (int et = 0; et < 2; ++et) o[t][et] = mfma16(pack8(av[t][et], av[t + 1][et]), pp, f32x4{0, 0, 0, 0});
    }
#pragma unroll
    for (int t = 0; t < 2; ++t)
#pragma unroll
      for (int et = 0; et < 2; ++et) {
        bf16x4 ob;
#pragma unroll
        for (int j = 0; j < 4; ++j) ob[j] = (__bf16)o[t][et][j];
        bf16* dp = outq + (long)(srow + 16 * t) * DM + h * 28 + 16 * et + 4 * g;
        if (et == 1) dp = (g < 3) ? dp : dummy + lane * 4;
        *(bf16x4*)dp = ob;
      }
  } else {
#pragma unroll
    for (int t = 0; t < 2; ++t)
#pragma unroll
      for (int c = 0; c < 2; ++c) {
        bf16x4 ob;
        f32x4 pv = phi4(aq[t][c]);
#pragma unroll
        for (int j = 0; j < 4; ++j) ob[j] = (__bf16)pv[j];
        bf16* dp = outq + (long)(srow + 16 * t) * DM + h * 28 + 16 * c + 4 * g;
        if (c == 1) dp = (g < 3) ? dp : dummy + lane * 4;
        *(bf16x4*)dp = ob;
      }
#pragma unroll
    for (int t = 1; t < 3; ++t) {
      ak[t][0] = phi4(ak[t][0]);
      ak[t][1] = phi4(ak[t][1]);
#pragma unroll
      for (int j = 0; j < 4; ++j)
        if (c16 >= 12) av[t][1][j] = (c16 == 12) ? 1.0f : 0.0f;
    }
    f32x4 kv[2][2];
#pragma unroll
    for (int dc = 0; dc < 2; ++dc)
#pragma unroll
      for (int ec = 0; ec < 2; ++ec)
        kv[dc][ec] = mfma16(pack8(ak[1][dc], ak[2][dc]), pack8(av[1][ec], av[2][ec]), f32x4{0, 0, 0, 0});
    BARRIER();
    f32x4* ex = (f32x4*)(lds) + hh * 1024 + lane;
    if (rq != 0) {
#pragma unroll
      for (int i = 0; i < 4; ++i) ex[(rq * 4 + i) * 64] = kv[i >> 1][i & 1];
    }
    BARRIER();
    if (rq == 0) {
      float* pb = part + ((long)rt * 16 + h) * 1024;
#pragma unroll
      for (int i = 0; i < 4; ++i) {
        f32x4 o = ((kv[i >> 1][i & 1] + ex[(4 + i) * 64]) + ex[(8 + i) * 64]) + ex[(12 + i) * 64];
        *(f32x4*)(pb + (16 * (i & 1) + c16) * 32 + 16 * (i >> 1) + 4 * g) = o;
      }
    }
  }
}

__global__ __launch_bounds__(768, 3) void k1_qkv(const bf16* __restrict__ xa, const bf16* __restrict__ wa,
                                                 const bf16* __restrict__ xb, const bf16* __restrict__ wb,
                                                 bf16* __restrict__ pq, bf16* __restrict__ ob,
                                                 float* __restrict__ part, bf16* __restrict__ dummy,
                                                 unsigned* __restrict__ flags) {
  extern __shared__ __attribute__((aligned(16))) char lds[];
  const int b = blockIdx.x;
  const int lane = threadIdx.x & 63;
  const int wv = __builtin_amdgcn_readfirstlane(threadIdx.x >> 6);
  const int xcd = b & 7, idx = b >> 3;
  const int br = xcd & 1, sub = xcd >> 1;
  const int rt = (sub >> 1) * 8 + (idx >> 2), hg = (sub & 1) * 4 + (idx & 3);
  if (wv >= 8) {
    if (wv == 8) flags[b * 64 + lane] = 0u;
    k1_loader(br ? xb : xa, br ? wb : wa, rt, hg, lds, wv - 8, lane);
    return;
  }
  if (br == 0) k1_body<0>(pq, part, dummy, rt, hg, lds, wv, lane);
  else         k1_body<1>(ob, part, dummy, rt, hg, lds, wv, lane);
  STAMP(0,5);
}

constexpr int OP_OB = 7 * 64 * 128;
constexpr int OP_WB = 448 * 224;
constexpr int OP_LDS = OP_OB + OP_WB;

template <int NW>
DI void op_issue(const bf16* __restrict__ a, const bf16* __restrict__ wo, int m0, int n0, int wv, int lane,
                 unsigned ldsbase, bool branch_a) {
  int woffs[NW];
#pragma unroll
  for (int i = 0; i < NW; ++i) {
    int c = (wv + 8 * i) * 64 + lane;
    int lrow = c / 14, pos = c - lrow * 14;
    woffs[i] = (((lrow & 32) + pi_inv(lrow & 31)) * DM + n0 + pos * 8) * 2;
  }
  const int row = wv * 8 + (lane >> 3), pos = lane & 7;
  const int aoff = ((m0 + row) * DM + ((pos ^ (row & 7)) * 8)) * 2;
  if (branch_a) {
#pragma unroll
    for (int kc = 0; kc < 7; ++kc) glds16((const char*)a + aoff + kc * 128, ldsbase + kc * 8192 + wv * 1024);
#pragma unroll
    for (int kc = 0; kc < 7; ++kc)
#pragma unroll
      for (int i = 0; i < NW; ++i)
        glds16((const char*)wo + woffs[i] + kc * (64 * DM * 2), ldsbase + OP_OB + kc * 14336 + (wv + 8 * i) * 1024);
  } else {
#pragma unroll
    for (int kc = 0; kc < 7; ++kc) {
      glds16((const char*)a + aoff + kc * 128, ldsbase + kc * 8192 + wv * 1024);
#pragma unroll
      for (int i = 0; i < NW; ++i)
        glds16((const char*)wo + woffs[i] + kc * (64 * DM * 2), ldsbase + OP_OB + kc * 14336 + (wv + 8 * i) * 1024);
    }
  }
}
template <int NPC> DI void chunk_wait(int kc) {
  switch (kc) {
    case 0: if (NPC == 3) WAIT_VM(18); else WAIT_VM(12); break;
    case 1: if (NPC == 3) WAIT_VM(15); else WAIT_VM(10); break;
    case 2: if (NPC == 3) WAIT_VM(12); else WAIT_VM(8); break;
    case 3: if (NPC == 3) WAIT_VM(9); else WAIT_VM(6); break;
    case 4: if (NPC == 3) WAIT_VM(6); else WAIT_VM(4); break;
    case 5: if (NPC == 3) WAIT_VM(3); else WAIT_VM(2); break;
    default: WAIT_VM(0); break;
  }
}

template <int NC, bool CHUNK_BARRIERS>
DI void op_compute(const char* lds, bf16* __restrict__ out, int m0, int ncol0, int ct0, int rbase, int lane) {
  const int g = lane >> 4, c16 = lane & 15;
  f32x4 acc[2][NC];
#pragma unroll
  for (int r = 0; r < 2; ++r)
#pragma unroll
    for (int c = 0; c < NC; ++c) acc[r][c] = f32x4{0, 0, 0, 0};
  const int ooff = (rbase + c16) * 128;
  const int woff = OP_OB + (4 * g + (c16 >> 2)) * 224 + ct0 * 32 + (c16 & 3) * 8;
#pragma unroll
  for (int ks = 0; ks < 14; ++ks) {
    if (CHUNK_BARRIERS && (ks & 1) == 0) { chunk_wait<3>(ks >> 1); BARRIER(); }
    bf16x8 of[2], wf[NC];
    const int xs = (ks >> 1) * 8192 + ((((ks & 1) * 4 + g) ^ (c16 & 7)) << 4);
#pragma unroll
    for (int r = 0; r < 2; ++r) of[r] = *(const bf16x8*)(lds + ooff + r * 2048 + xs);
#pragma unroll
    for (int c = 0; c < NC; ++c) wf[c] = tr_frag(lds + woff + ks * 32 * 224 + c * 32, 16 * 224);
#pragma unroll
    for (int r = 0; r < 2; ++r)
#pragma unroll
      for (int c = 0; c < NC; ++c) acc[r][c] = mfma16(wf[c], of[r], acc[r][c]);
  }
#pragma unroll
  for (int r = 0; r < 2; ++r)
#pragma unroll
    for (int c = 0; c < NC; ++c) {
      bf16x4 ob;
#pragma unroll
      for (int j = 0; j < 4; ++j) ob[j] = (__bf16)acc[r][c][j];
      *(bf16x4*)(out + (long)(m0 + rbase + r * 16 + c16) * (2 * DM) + ncol0 + (ct0 + c) * 16 + 4 * g) = ob;
    }
}

#define RLX_AGENT __ATOMIC_RELAXED, __HIP_MEMORY_SCOPE_AGENT
typedef __attribute__((address_space(1))) unsigned gu32;
typedef __attribute__((address_space(1))) unsigned long long gu64;

DI bf16x4 reduce_head(const float* __restrict__ part, int h, int tid) {
  const float* p = part + (long)h * 1024 + tid * 4;
  f32x4 v[16];
#pragma unroll
  for (int i = 0; i < 16; ++i) v[i] = *(const f32x4*)(p + (long)i * 16 * 1024);
  f32x4 s = v[0];
#pragma unroll
  for (int i = 1; i < 16; ++i) s += v[i];
  const int e = tid >> 3, d = (tid & 7) * 4;
  bf16x4 r;
#pragma unroll
  for (int j = 0; j < 4; ++j) r[j] = (__bf16)((e <= 28 && d + j < 28) ? s[j] : 0.f);
  return r;
}

__global__ __launch_bounds__(512, 2) void k2_out(const bf16* __restrict__ pq, const bf16* __restrict__ woa,
                                                 const bf16* __restrict__ ob, const bf16* __restrict__ wob,
                                                 const float* __restrict__ part, bf16* kvt,
                                                 unsigned* flags, bf16* slow, bf16* __restrict__ out) {
  extern __shared__ __attribute__((aligned(16))) char lds[];
  const int tid = threadIdx.x, lane = tid & 63;
  const int wv = __builtin_amdgcn_readfirstlane(tid >> 6);
  const int g = lane >> 4, c16 = lane & 15;
  const int b = blockIdx.x;
  const int xcd = b & 7, idx = b >> 3;
  const int br = xcd & 1, sub = xcd >> 1;
  const int mt = sub * 8 + (idx >> 2), nt = idx & 3;
  const int m0 = mt * 64, n0 = nt * 112;
  const unsigned ldsbase = (unsigned)(size_t)lds;
  STAMP(1,0);
  if (br == 1) {
    if (idx < 4 && wv < 4) {
      const int h = sub * 4 + idx;
      bf16x4 r = reduce_head(part, h, tid);
      const int e = tid >> 3, d = (tid & 7) * 4;
      const unsigned long long v = __builtin_bit_cast(unsigned long long, r);
      gu64* gp = (gu64*)flags + (((h * 2 + (e >> 4)) * 4 + ((d & 7) >> 1)) * 64 + (d >> 3) * 16 + (e & 15));
      __hip_atomic_store(gp, (1ull << 32) | (v & 0xffffffffull), RLX_AGENT);
      __hip_atomic_store(gp + 64, (1ull << 32) | (v >> 32), RLX_AGENT);
    }
    if (wv < 6) op_issue<2>(ob, wob, m0, n0, wv, lane, ldsbase, false);
    else        op_issue<1>(ob, wob, m0, n0, wv, lane, ldsbase, false);
    STAMP(1,1);
    if (wv < 2)      op_compute<4, true>(lds, out, m0, DM + n0, 0, (wv & 1) * 32, lane);
    else if (wv < 4) op_compute<3, true>(lds, out, m0, DM + n0, 4, (wv & 1) * 32, lane);
    else if (wv < 6) {
#pragma unroll
      for (int kc = 0; kc < 7; ++kc) { chunk_wait<3>(kc); __builtin_amdgcn_s_barrier(); }
    } else {
#pragma unroll
      for (int kc = 0; kc < 7; ++kc) { chunk_wait<2>(kc); __builtin_amdgcn_s_barrier(); }
    }
    STAMP(1,4);
    return;
  }
  if (wv < 6) { op_issue<2>(pq, woa, m0, n0, wv, lane, ldsbase, true); WAIT_VM(14); }
  else        { op_issue<1>(pq, woa, m0, n0, wv, lane, ldsbase, true); WAIT_VM(7); }
  BARRIER();
  STAMP(1,1);
  bf16x8 qf[2][4];
#pragma unroll
  for (int hi = 0; hi < 2; ++hi)
#pragma unroll
    for (int t = 0; t < 4; ++t) {
      const int row = 16 * t + c16;
      const int col = (wv * 2 + hi) * 28 + 8 * g, col1 = col + 4;
      bf16x4 lo = *(const bf16x4*)(lds + (col >> 6) * 8192 + row * 128 + ((((col & 63) >> 3) ^ (row & 7)) << 4) + (col & 7) * 2);
      int a1 = (col1 >> 6) * 8192 + row * 128 + ((((col1 & 63) >> 3) ^ (row & 7)) << 4) + (col1 & 7) * 2;
      bf16x4 hi4 = *(const bf16x4*)(lds + a1);
      if (g == 3) { hi4[0] = (__bf16)0.f; hi4[1] = (__bf16)0.f; hi4[2] = (__bf16)0.f; hi4[3] = (__bf16)0.f; }
      qf[hi][t] = __builtin_shufflevector(lo, hi4, 0, 1, 2, 3, 4, 5, 6, 7);
    }
  bf16x8 kf[2][2];
  {
    const gu64* gp = (const gu64*)flags + (wv * 16) * 64 + lane;
    unsigned val[16];
    bool okall = false;
    for (int spin = 0; spin < 4000; ++spin) {
      bool ok = true;
#pragma unroll
      for (int i = 0; i < 16; ++i) {
        unsigned long long x = __hip_atomic_load(gp + i * 64, RLX_AGENT);
        val[i] = (unsigned)x;
        ok = ok && ((x >> 32) == 1ull);
      }
      if (__all(ok)) { okall = true; break; }
      __builtin_amdgcn_s_sleep(2);
    }
    typedef __attribute__((ext_vector_type(4))) unsigned u32x4;
    if (okall) {
#pragma unroll
      for (int q = 0; q < 4; ++q)
        kf[q >> 1][q & 1] = __builtin_bit_cast(bf16x8, u32x4{val[4 * q], val[4 * q + 1], val[4 * q + 2], val[4 * q + 3]});
    } else {
      bf16* mine = slow + (long)b * 16384;
#pragma unroll 1
      for (int i = 0; i < 8; ++i) {
        const int h = wv * 2 + (i >> 2), t4 = (i & 3) * 64 + lane;
        *(bf16x4*)(mine + h * 1024 + t4 * 4) = reduce_head(part, h, t4);
      }
      asm volatile("s_waitcnt vmcnt(0)" ::: "memory");
#pragma unroll
      for (int hi = 0; hi < 2; ++hi) {
        kf[hi][0] = *(const bf16x8*)(mine + (wv * 2 + hi) * 1024 + c16 * 32 + 8 * g);
        kf[hi][1] = *(const bf16x8*)(mine + (wv * 2 + hi) * 1024 + (16 + c16) * 32 + 8 * g);
      }
    }
  }
  STAMP(1,2);
#pragma unroll
  for (int hi = 0; hi < 2; ++hi) {
    const int h = wv * 2 + hi;
    f32x4 n0v[4], n1v[4];
#pragma unroll
    for (int t = 0; t < 4; ++t) {
      n0v[t] = mfma16(kf[hi][0], qf[hi][t], f32x4{0, 0, 0, 0});
      n1v[t] = mfma16(kf[hi][1], qf[hi][t], f32x4{0, 0, 0, 0});
    }
#pragma unroll
    for (int t = 0; t < 4; ++t) {
      float den = bcast_row3(n1v[t][0]);
      den = bfr(bfr(den) + 1e-6f);
      const float inv = 1.0f / den;
      bf16x4 o0, o1;
      f32x4 r0 = bfr4(n0v[t]) * inv, r1 = bfr4(n1v[t]) * inv;
#pragma unroll
      for (int j = 0; j < 4; ++j) { o0[j] = (__bf16)r0[j]; o1[j] = (__bf16)r1[j]; }
      const int row = 16 * t + c16;
      const int col = h * 28 + 4 * g;
      *(bf16x4*)(lds + (col >> 6) * 8192 + row * 128 + ((((col & 63) >> 3) ^ (row & 7)) << 4) + (col & 7) * 2) = o0;
      const int col1 = col + 16;
      int a1 = (col1 >> 6) * 8192 + row * 128 + ((((col1 & 63) >> 3) ^ (row & 7)) << 4) + (col1 & 7) * 2;
      a1 = (g < 3) ? a1 : OP_LDS + 16 + lane * 8;
      *(bf16x4*)(lds + a1) = o1;
    }
  }
  asm volatile("s_waitcnt vmcnt(0)" ::: "memory");
  BARRIER();
  STAMP(1,3);
  if (wv < 2)      op_compute<4, false>(lds, out, m0, n0, 0, (wv & 1) * 32, lane);
  else if (wv < 4) op_compute<3, false>(lds, out, m0, n0, 4, (wv & 1) * 32, lane);
  STAMP(1,4);
}

extern "C" void kernel_launch(void* const* d_in, const int* in_sizes, int n_in,
                              void* d_out, int out_size, void* d_ws, size_t ws_size,
                              hipStream_t stream) {
  const bf16* xa = (const bf16*)d_in[0];
  const bf16* wqa = (const bf16*)d_in[1];
  const bf16* woa = (const bf16*)d_in[2];
  const bf16* xb = (const bf16*)d_in[3];
  const bf16* wqb = (const bf16*)d_in[4];
  const bf16* wob = (const bf16*)d_in[5];
  bf16* out = (bf16*)d_out;
  char* ws = (char*)d_ws;
  bf16* pq = (bf16*)ws;
  bf16* ob = (bf16*)(ws + 2 * 1024 * 1024);
  float* part = (float*)(ws + 4 * 1024 * 1024);
  bf16* kvt = (bf16*)(ws + 6 * 1024 * 1024);
  unsigned* flags = (unsigned*)(ws + 7 * 1024 * 1024);
  bf16* dummy = (bf16*)(ws + 7 * 1024 * 1024 + 131072);
  bf16* slow = (bf16*)(ws + 8 * 1024 * 1024);

  (void)hipFuncSetAttribute((const void*)k1_qkv, hipFuncAttributeMaxDynamicSharedMemorySize, K1_LDS);
  (void)hipFuncSetAttribute((const void*)k2_out, hipFuncAttributeMaxDynamicSharedMemorySize, OP_LDS + 1024);

  k1_qkv<<<256, 768, K1_LDS, stream>>>(xa, wqa, xb, wqb, pq, ob, part, dummy, flags);
  k2_out<<<256, 512, OP_LDS + 1024, stream>>>(pq, woa, ob, wob, part, kvt, flags, slow, out);
}
```

```cpp
#include <hip/hip_runtime.h>
#include <hip/hip_bf16.h>
#include <stdint.h>

typedef __bf16 bf16;
typedef __attribute__((ext_vector_type(8))) __bf16 bf16x8;
typedef __attribute__((ext_vector_type(4))) __bf16 bf16x4;
typedef __attribute__((ext_vector_type(4))) float f32x4;
#define LAS __attribute__((address_space(3)))
#define DI __device__ __forceinline__

#define STAMP(k,i) do {} while (0)
constexpr int SEQ = 2048, DM = 448, NQKV = 1344;

DI float bfr(float x) { return (float)(__bf16)x; }

typedef __attribute__((ext_vector_type(2))) __bf16 bf16x2;
DI f32x4 bfr4(f32x4 v) {
  bf16x2 a = {(__bf16)v[0], (__bf16)v[1]}, b = {(__bf16)v[2], (__bf16)v[3]};
  unsigned ua = __builtin_bit_cast(unsigned, a), ub = __builtin_bit_cast(unsigned, b);
  asm volatile("" : "+v"(ua), "+v"(ub));
  return f32x4{__uint_as_float(ua << 16), __uint_as_float(ua & 0xffff0000u), __uint_as_float(ub << 16), __uint_as_float(ub & 0xffff0000u)};
}
DI void glds16(const void* gsrc, unsigned lds_dst) {
  unsigned keep;
  asm volatile("s_mov_b32 %0, m0\n\ts_mov_b32 m0, %2\n\ts_nop 0\n\tglobal_load_lds_dwordx4 %1, off\n\ts_mov_b32 m0, %0"
               : "=&s"(keep) : "v"(gsrc), "s"(lds_dst) : "memory");
}
#define WAIT_VM(n) asm volatile("s_waitcnt vmcnt(" #n ")" ::: "memory")
#define BARRIER() do { asm volatile("s_waitcnt lgkmcnt(0)" ::: "memory"); __builtin_amdgcn_s_barrier(); asm volatile("" ::: "memory"); } while (0)

DI bf16x8 tr_frag(const char* p, int hi_off) {
  bf16x4 lo = __builtin_amdgcn_ds_read_tr16_b64_v4bf16((LAS bf16x4*)(p));
  bf16x4 hi = __builtin_amdgcn_ds_read_tr16_b64_v4bf16((LAS bf16x4*)(p + hi_off));
  return __builtin_shufflevector(lo, hi, 0, 1, 2, 3, 4, 5, 6, 7);
}
DI f32x4 mfma16(bf16x8 a, bf16x8 b, f32x4 c) { return __builtin_amdgcn_mfma_f32_16x16x32_bf16(a, b, c, 0, 0, 0); }

DI bf16x8 pack8(f32x4 a, f32x4 b) {
  bf16x8 r;
#pragma unroll
  for (int i = 0; i < 4; ++i) { r[i] = (__bf16)a[i]; r[4 + i] = (__bf16)b[i]; }
  return r;
}
DI float phi_f(float t) {
  float r = t > 0.f ? t + 1.0f : bfr(__expf(t)) + 1e-6f;
  return bfr(r);
}
DI f32x4 phi4(f32x4 acc) {
  f32x4 t = bfr4(acc), e;
#pragma unroll
  for (int j = 0; j < 4; ++j) e[j] = t[j] > 0.f ? t[j] + 1.0f : __expf(t[j]) + 1e-6f;
  return e;
}
DI float xmax16_32(float m) {
  unsigned u = __float_as_uint(m);
  auto r = __builtin_amdgcn_permlane16_swap(u, u, false, false);
  float a = fmaxf(__uint_as_float(r[0]), __uint_as_float(r[1]));
  unsigned v = __float_as_uint(a);
  auto r2 = __builtin_amdgcn_permlane32_swap(v, v, false, false);
  return fmaxf(__uint_as_float(r2[0]), __uint_as_float(r2[1]));
}
DI float xsum16_32(float m) {
  unsigned u = __float_as_uint(m);
  auto r = __builtin_amdgcn_permlane16_swap(u, u, false, false);
  float a = __uint_as_float(r[0]) + __uint_as_float(r[1]);
  unsigned v = __float_as_uint(a);
  auto r2 = __builtin_amdgcn_permlane32_swap(v, v, false, false);
  return __uint_as_float(r2[0]) + __uint_as_float(r2[1]);
}
DI float bcast_row3(float x) {
  unsigned u = __float_as_uint(x);
  auto r = __builtin_amdgcn_permlane32_swap(u, u, false, false);
  unsigned v = r[1];
  auto r2 = __builtin_amdgcn_permlane16_swap(v, v, false, false);
  return __uint_as_float(r2[1]);
}
template <int NM, int ND> DI void interleave() {
  constexpr int LEAD = ND > NM ? ND - NM : 0;
  if (LEAD) __builtin_amdgcn_sched_group_barrier(0x100, LEAD, 0);
#pragma unroll
  for (int i = 0; i < NM; ++i) {
    __builtin_amdgcn_sched_group_barrier(0x008, 1, 0);
    if (i + LEAD < ND) __builtin_amdgcn_sched_group_barrier(0x100, 1, 0);
  }
}
DI int pi_inv(int lr) { return lr < 16 ? 8 * (lr >> 2) + (lr & 3) : 8 * ((lr - 16) >> 2) + 4 + (lr & 3); }

constexpr int K1_XB = 144 * 128;
constexpr int K1_WB = 64 * 352;
constexpr int K1_STAGE = K1_XB + K1_WB;
constexpr int K1_LDS = 3 * K1_STAGE;

DI void k1_loader(const bf16* __restrict__ x, const bf16* __restrict__ w, int rt, int hg, char* lds, int wv, int lane) {
  const int s0 = rt * 128;
  const unsigned ldsbase = (unsigned)(size_t)lds;
  const char* src[10];
  unsigned dst[10];
  long strd0;
#pragma unroll
  for (int i = 0; i < 4; ++i) {
    int xi = 2 + wv + 4 * i;
    int row = xi * 8 + (lane >> 3), pos = lane & 7;
    int grow = s0 - 16 + row;
    src[i] = (const char*)(x + (long)grow * DM + ((pos ^ (row & 7)) * 8));
    dst[i] = xi * 1024;
  }
#pragma unroll
  for (int i = 0; i < 6; ++i) {
    int j = wv + 4 * i;
    if (i == 0 && j < 2) {
      int row = j * 8 + (lane >> 3), pos = lane & 7;
      int grow = s0 - 16 + row; grow = grow < 0 ? 0 : grow;
      src[4] = (const char*)(x + (long)grow * DM + ((pos ^ (row & 7)) * 8));
      dst[4] = j * 1024;
      strd0 = 128;
    } else {
      int wi = j - 2;
      int c = wi * 64 + lane;
      int lrow = c / 22, pos = c - lrow * 22;
      pos = pos > 20 ? 20 : pos;
      int k = (lrow & 32) + pi_inv(lrow & 31);
      int seg = pos / 7, cc = pos - seg * 7;
      src[4 + i] = (const char*)(w + (long)k * NQKV + seg * DM + hg * 56 + cc * 8);
      dst[4 + i] = K1_XB + wi * 1024;
      if (i == 0) strd0 = 64L * NQKV * 2;
    }
  }
  auto issue = [&](int st, int buf) {
    unsigned b = ldsbase + buf * K1_STAGE;
#pragma unroll
    for (int i = 0; i < 4; ++i) glds16(src[i] + st * 128, b + dst[i]);
    glds16(src[4] + st * strd0, b + dst[4]);
#pragma unroll
    for (int i = 5; i < 10; ++i) glds16(src[i] + (long)st * (64L * NQKV * 2), b + dst[i]);
  };
  issue(0, 0);
  issue(1, 1);
#pragma unroll 1
  for (int st = 0; st < 7; ++st) {
    if (st < 6) WAIT_VM(10); else WAIT_VM(0);
    __builtin_amdgcn_s_barrier();
    if (st + 2 < 7) issue(st + 2, (st + 2) % 3);
  }
}

template <int BR>
DI void k1_body(bf16* __restrict__ outq, float* __restrict__ part, bf16* __restrict__ dummy, int rt, int hg, char* lds,
                int wv, int lane) {
  const int rq = wv >> 1, hh = wv & 1;
  const int g = lane >> 4, c16 = lane & 15;
  const int s0 = rt * 128;

  constexpr int T0 = BR ? 0 : 1;
  f32x4 aq[2][2], ak[3][2], av[3][2];
#pragma unroll
  for (int t = 0; t < 3; ++t)
#pragma unroll
    for (int c = 0; c < 2; ++c) {
      if (t < 2) aq[t][c] = f32x4{0, 0, 0, 0};
      ak[t][c] = f32x4{0, 0, 0, 0}; av[t][c] = f32x4{0, 0, 0, 0};
    }

  STAMP(0,0);
  const int xoff = (rq * 32 + c16) * 128;
  const int woff = K1_XB + (4 * g + (c16 >> 2)) * 352 + hh * 56 + (c16 & 3) * 8;
  struct Frags { bf16x8 xf[3], wf[6]; };
  auto ldf = [&](Frags& f, const char* sb, int ks) {
    const int xs = ((ks * 4 + g) ^ (c16 & 7)) << 4;
#pragma unroll
    for (int ct = 0; ct < 6; ++ct)
      f.wf[ct] = tr_frag(sb + woff + ks * 32 * 352 + (ct >> 1) * 112 + (ct & 1) * 32, 16 * 352);
#pragma unroll
    for (int t = T0; t < 3; ++t) f.xf[t] = *(const bf16x8*)(sb + xoff + t * 2048 + xs);
  };
  auto mma = [&](const Frags& f) {
#pragma unroll
    for (int t = T0; t < 3; ++t) {
#pragma unroll
      for (int c = 0; c < 2; ++c) {
        if (t >= 1) aq[t - 1][c] = mfma16(f.wf[c], f.xf[t], aq[t - 1][c]);
        if (BR) ak[t][c] = mfma16(f.wf[2 + c], f.xf[t], ak[t][c]);
        else    ak[t][c] = mfma16(f.xf[t], f.wf[2 + c], ak[t][c]);
        av[t][c] = mfma16(f.xf[t], f.wf[4 + c], av[t][c]);
      }
    }
  };
  Frags fa, fb;
  BARRIER();
  STAMP(0,2);
  ldf(fa, lds, 0);
  int buf = 0;
#pragma unroll 1
  for (int st = 0; st < 6; ++st) {
    if (st == 3) STAMP(0,3);
    ldf(fb, lds + buf * K1_STAGE, 1);
    mma(fa);
    interleave<BR ? 16 : 12, BR ? 15 : 14>();
    __builtin_amdgcn_sched_barrier(0);
    buf = buf == 2 ? 0 : buf + 1;
    BARRIER();
    ldf(fa, lds + buf * K1_STAGE, 0);
    mma(fb);
    interleave<BR ? 16 : 12, BR ? 15 : 14>();
    __builtin_amdgcn_sched_barrier(0);
  }
  ldf(fb, lds + buf * K1_STAGE, 1);
  mma(fa);
  interleave<BR ? 16 : 12, BR ? 15 : 14>();
  __builtin_amdgcn_sched_barrier(0);
  mma(fb);

  STAMP(0,4);
  const int h = hg * 2 + hh;
  const int srow = s0 + rq * 32 + c16;
  if (BR) {
    const float scale = 0.18898223650461362f;
    bf16x8 kp[3], qp[2];
#pragma unroll
    for (int t = 0; t < 3; ++t) {
      f32x4 hi = ak[t][1];
      if (g == 3) hi = f32x4{0, 0, 0, 0};
      kp[t] = pack8(ak[t][0], hi);
    }
#pragma unroll
    for (int t = 0; t < 2; ++t) {
      f32x4 hi = aq[t][1];
      if (g == 3) hi = f32x4{0, 0, 0, 0};
      qp[t] = pack8(aq[t][0], hi);
    }
    f32x4 sp[2], sc[2];
#pragma unroll
    for (int t = 0; t < 2; ++t) {
      sp[t] = mfma16(kp[t], qp[t], f32x4{0, 0, 0, 0});
      sc[t] = mfma16(kp[t + 1], qp[t], f32x4{0, 0, 0, 0});
    }
    bool okp[4], okc[4];
#pragma unroll
    for (int j = 0; j < 4; ++j) {
      int dp = c16 + 16 - 4 * g - j, dc = c16 - 4 * g - j;
      okp[j] = ((dp & 1) == 0) && dp <= 14;
      okc[j] = dc >= 0 && ((dc & 1) == 0);
    }
    const bool first = (s0 + rq * 32) == 0;
    const float NEG = -__builtin_inff();
    float m[2];
#pragma unroll
    for (int t = 0; t < 2; ++t) {
#pragma unroll
      for (int j = 0; j < 4; ++j) {
        bool op = okp[j] && !(t == 0 && first);
        sp[t][j] = op ? sp[t][j] : NEG;
        sc[t][j] = okc[j] ? sc[t][j] : NEG;
      }
      m[t] = fmaxf(fmaxf(fmaxf(sp[t][0], sp[t][1]), fmaxf(sp[t][2], sp[t][3])),
                   fmaxf(fmaxf(sc[t][0], sc[t][1]), fmaxf(sc[t][2], sc[t][3])));
    }
#pragma unroll
    for (int t = 0; t < 2; ++t) m[t] = xmax16_32(m[t]);
    const float c2 = scale * 1.4426950408889634f;
    float sum[2];
#pragma unroll
    for (int t = 0; t < 2; ++t) {
      const float mc = -m[t] * c2;
#pragma unroll
      for (int j = 0; j < 4; ++j) {
        sp[t][j] = __builtin_amdgcn_exp2f(fmaf(sp[t][j], c2, mc));
        sc[t][j] = __builtin_amdgcn_exp2f(fmaf(sc[t][j], c2, mc));
      }
      f32x4 sv = sp[t] + sc[t];
      sum[t] = (sv[0] + sv[1]) + (sv[2] + sv[3]);
    }
#pragma unroll
    for (int t = 0; t < 2; ++t) sum[t] = __builtin_amdgcn_rcpf(xsum16_32(sum[t]));
    f32x4 o[2][2];
#pragma unroll
    for (int t = 0; t < 2; ++t) {
      bf16x8 pp = pack8(sp[t] * sum[t], sc[t] * sum[t]);
#pragma unroll
      for (int et = 0; et < 2; ++et) o[t][et] = mfma16(pack8(av[t][et], av[t + 1][et]), pp, f32x4{0, 0, 0, 0});
    }
#pragma unroll
    for (int t = 0; t < 2; ++t)
#pragma unroll
      for (int et = 0; et < 2; ++et) {
        bf16x4 ob;
#pragma unroll
        for (int j = 0; j < 4; ++j) ob[j] = (__bf16)o[t][et][j];
        bf16* dp = outq + (long)(srow + 16 * t) * DM + h * 28 + 16 * et + 4 * g;
        if (et == 1) dp = (g < 3) ? dp : dummy + lane * 4;
        *(bf16x4*)dp = ob;
      }
  } else {
#pragma unroll
    for (int t = 0; t < 2; ++t)
#pragma unroll
      for (int c = 0; c < 2; ++c) {
        bf16x4 ob;
        f32x4 pv = phi4(aq[t][c]);
#pragma unroll
        for (int j = 0; j < 4; ++j) ob[j] = (__bf16)pv[j];
        bf16* dp = outq + (long)(srow + 16 * t) * DM + h * 28 + 16 * c + 4 * g;
        if (c == 1) dp = (g < 3) ? dp : dummy + lane * 4;
        *(bf16x4*)dp = ob;
      }
#pragma unroll
    for (int t = 1; t < 3; ++t) {
      ak[t][0] = phi4(ak[t][0]);
      ak[t][1] = phi4(ak[t][1]);
#pragma unroll
      for (int j = 0; j < 4; ++j)
        if (c16 >= 12) av[t][1][j] = (c16 == 12) ? 1.0f : 0.0f;
    }
    f32x4 kv[2][2];
#pragma unroll
    for (int dc = 0; dc < 2; ++dc)
#pragma unroll
      for (int ec = 0; ec < 2; ++ec)
        kv[dc][ec] = mfma16(pack8(ak[1][dc], ak[2][dc]), pack8(av[1][ec], av[2][ec]), f32x4{0, 0, 0, 0});
    BARRIER();
    f32x4* ex = (f32x4*)(lds) + hh * 1024 + lane;
    if (rq != 0) {
#pragma unroll
      for (int i = 0; i < 4; ++i) ex[(rq * 4 + i) * 64] = kv[i >> 1][i & 1];
    }
    BARRIER();
    if (rq == 0) {
      float* pb = part + ((long)rt * 16 + h) * 1024;
#pragma unroll
      for (int i = 0; i < 4; ++i) {
        f32x4 o = ((kv[i >> 1][i & 1] + ex[(4 + i) * 64]) + ex[(8 + i) * 64]) + ex[(12 + i) * 64];
        *(f32x4*)(pb + (16 * (i & 1) + c16) * 32 + 16 * (i >> 1) + 4 * g) = o;
      }
    }
  }
}

__global__ __launch_bounds__(768, 3) void k1_qkv(const bf16* __restrict__ xa, const bf16* __restrict__ wa,
                                                 const bf16* __restrict__ xb, const bf16* __restrict__ wb,
                                                 bf16* __restrict__ pq, bf16* __restrict__ ob,
                                                 float* __restrict__ part, bf16* __restrict__ dummy,
                                                 unsigned* __restrict__ flags) {
  extern __shared__ __attribute__((aligned(16))) char lds[];
  const int b = blockIdx.x;
  const int lane = threadIdx.x & 63;
  const int wv = __builtin_amdgcn_readfirstlane(threadIdx.x >> 6);
  const int xcd = b & 7, idx = b >> 3;
  const int br = xcd & 1, sub = xcd >> 1;
  const int rt = (sub >> 1) * 8 + (idx >> 2), hg = (sub & 1) * 4 + (idx & 3);
  if (wv >= 8) {
    if (wv == 8) flags[b * 64 + lane] = 0u;
    k1_loader(br ? xb : xa, br ? wb : wa, rt, hg, lds, wv - 8, lane);
    return;
  }
  if (br == 0) k1_body<0>(pq, part, dummy, rt, hg, lds, wv, lane);
  else         k1_body<1>(ob, part, dummy, rt, hg, lds, wv, lane);
  STAMP(0,5);
}

constexpr int OP_OB = 7 * 64 * 128;
constexpr int OP_WB = 448 * 224;
constexpr int OP_LDS = OP_OB + OP_WB;

template <int NW>
DI void op_issue(const bf16* __restrict__ a, const bf16* __restrict__ wo, int m0, int n0, int wv, int lane,
                 unsigned ldsbase, bool branch_a) {
  int woffs[NW];
#pragma unroll
  for (int i = 0; i < NW; ++i) {
    int c = (wv + 8 * i) * 64 + lane;
    int lrow = c / 14, pos = c - lrow * 14;
    woffs[i] = (((lrow & 32) + pi_inv(lrow & 31)) * DM + n0 + pos * 8) * 2;
  }
  const int row = wv * 8 + (lane >> 3), pos = lane & 7;
  const int aoff = ((m0 + row) * DM + ((pos ^ (row & 7)) * 8)) * 2;
  if (branch_a) {
#pragma unroll
    for (int kc = 0; kc < 7; ++kc) glds16((const char*)a + aoff + kc * 128, ldsbase + kc * 8192 + wv * 1024);
#pragma unroll
    for (int kc = 0; kc < 7; ++kc)
#pragma unroll
      for (int i = 0; i < NW; ++i)
        glds16((const char*)wo + woffs[i] + kc * (64 * DM * 2), ldsbase + OP_OB + kc * 14336 + (wv + 8 * i) * 1024);
  } else {
#pragma unroll
    for (int kc = 0; kc < 7; ++kc) {
      glds16((const char*)a + aoff + kc * 128, ldsbase + kc * 8192 + wv * 1024);
#pragma unroll
      for (int i = 0; i < NW; ++i)
        glds16((const char*)wo + woffs[i] + kc * (64 * DM * 2), ldsbase + OP_OB + kc * 14336 + (wv + 8 * i) * 1024);
    }
  }
}
template <int NC>
DI void op_compute(const char* lds, bf16* __restrict__ out, int m0, int ncol0, int ct0, int rbase, int lane) {
  const int g = lane >> 4, c16 = lane & 15;
  f32x4 acc[2][NC];
#pragma unroll
  for (int r = 0; r < 2; ++r)
#pragma unroll
    for (int c = 0; c < NC; ++c) acc[r][c] = f32x4{0, 0, 0, 0};
  const char* op = lds + (rbase + c16) * 128;
  const char* wp = lds + OP_OB + (4 * g + (c16 >> 2)) * 224 + ct0 * 32 + (c16 & 3) * 8;
  const int xs0 = (g ^ (c16 & 7)) << 4, xs1 = ((4 + g) ^ (c16 & 7)) << 4;
  struct Fr { bf16x8 of[2], wf[NC]; };
  auto ldf = [&](Fr& f, int xs, int wo) {
#pragma unroll
    for (int c = 0; c < NC; ++c) f.wf[c] = tr_frag(wp + wo + c * 32, 16 * 224);
#pragma unroll
    for (int r = 0; r < 2; ++r) f.of[r] = *(const bf16x8*)(op + r * 2048 + xs);
  };
  auto mma = [&](const Fr& f) {
#pragma unroll
    for (int r = 0; r < 2; ++r)
#pragma unroll
      for (int c = 0; c < NC; ++c) acc[r][c] = mfma16(f.wf[c], f.of[r], acc[r][c]);
  };
  Fr fa, fb;
  ldf(fa, xs0, 0);
#pragma unroll 1
  for (int kc = 0; kc < 6; ++kc) {
    ldf(fb, xs1, 32 * 224);
    mma(fa);
    __builtin_amdgcn_sched_barrier(0);
    op += 8192; wp += 64 * 224;
    ldf(fa, xs0, 0);
    mma(fb);
    __builtin_amdgcn_sched_barrier(0);
  }
  ldf(fb, xs1, 32 * 224);
  mma(fa);
  mma(fb);
#pragma unroll
  for (int r = 0; r < 2; ++r)
#pragma unroll
    for (int c = 0; c < NC; ++c) {
      bf16x4 ob;
#pragma unroll
      for (int j = 0; j < 4; ++j) ob[j] = (__bf16)acc[r][c][j];
      *(bf16x4*)(out + (long)(m0 + rbase + r * 16 + c16) * (2 * DM) + ncol0 + (ct0 + c) * 16 + 4 * g) = ob;
    }
}

#define RLX_AGENT __ATOMIC_RELAXED, __HIP_MEMORY_SCOPE_AGENT
typedef __attribute__((address_space(1))) unsigned gu32;
typedef __attribute__((address_space(1))) unsigned long long gu64;

DI bf16x4 reduce_head(const float* __restrict__ part, int h, int tid) {
  const float* p = part + (long)h * 1024 + tid * 4;
  f32x4 v[16];
#pragma unroll
  for (int i = 0; i < 16; ++i) v[i] = *(const f32x4*)(p + (long)i * 16 * 1024);
  f32x4 s = v[0];
#pragma unroll
  for (int i = 1; i < 16; ++i) s += v[i];
  const int e = tid >> 3, d = (tid & 7) * 4;
  bf16x4 r;
#pragma unroll
  for (int j = 0; j < 4; ++j) r[j] = (__bf16)((e <= 28 && d + j < 28) ? s[j] : 0.f);
  return r;
}

__global__ __launch_bounds__(512, 2) void k2_out(const bf16* __restrict__ pq, const bf16* __restrict__ woa,
                                                 const bf16* __restrict__ ob, const bf16* __restrict__ wob,
                                                 const float* __restrict__ part, bf16* kvt,
                                                 unsigned* flags, bf16* slow, bf16* __restrict__ out) {
  extern __shared__ __attribute__((aligned(16))) char lds[];
  const int tid = threadIdx.x, lane = tid & 63;
  const int wv = __builtin_amdgcn_readfirstlane(tid >> 6);
  const int g = lane >> 4, c16 = lane & 15;
  const int b = blockIdx.x;
  const int xcd = b & 7, idx = b >> 3;
  const int br = xcd & 1, sub = xcd >> 1;
  const int mt = sub * 8 + (idx >> 2), nt = idx & 3;
  const int m0 = mt * 64, n0 = nt * 112;
  const unsigned ldsbase = (unsigned)(size_t)lds;
  STAMP(1,0);
  if (br == 1) {
    if (idx < 4 && wv < 4) {
      const int h = sub * 4 + idx;
      bf16x4 r = reduce_head(part, h, tid);
      const int e = tid >> 3, d = (tid & 7) * 4;
      const unsigned long long v = __builtin_bit_cast(unsigned long long, r);
      gu64* gp = (gu64*)flags + (((h * 2 + (e >> 4)) * 4 + ((d & 7) >> 1)) * 64 + (d >> 3) * 16 + (e & 15));
      __hip_atomic_store(gp, (1ull << 32) | (v & 0xffffffffull), RLX_AGENT);
      __hip_atomic_store(gp + 64, (1ull << 32) | (v >> 32), RLX_AGENT);
    }
    if (wv < 6) op_issue<2>(ob, wob, m0, n0, wv, lane, ldsbase, false);
    else        op_issue<1>(ob, wob, m0, n0, wv, lane, ldsbase, false);
    STAMP(1,1);
  } else {
  if (wv < 6) { op_issue<2>(pq, woa, m0, n0, wv, lane, ldsbase, true); WAIT_VM(14); }
  else        { op_issue<1>(pq, woa, m0, n0, wv, lane, ldsbase, true); WAIT_VM(7); }
  BARRIER();
  STAMP(1,1);
  bf16x8 qf[2][4];
#pragma unroll
  for (int hi = 0; hi < 2; ++hi)
#pragma unroll
    for (int t = 0; t < 4; ++t) {
      const int row = 16 * t + c16;
      const int col = (wv * 2 + hi) * 28 + 8 * g, col1 = col + 4;
      bf16x4 lo = *(const bf16x4*)(lds + (col >> 6) * 8192 + row * 128 + ((((col & 63) >> 3) ^ (row & 7)) << 4) + (col & 7) * 2);
      int a1 = (col1 >> 6) * 8192 + row * 128 + ((((col1 & 63) >> 3) ^ (row & 7)) << 4) + (col1 & 7) * 2;
      bf16x4 hi4 = *(const bf16x4*)(lds + a1);
      if (g == 3) { hi4[0] = (__bf16)0.f; hi4[1] = (__bf16)0.f; hi4[2] = (__bf16)0.f; hi4[3] = (__bf16)0.f; }
      qf[hi][t] = __builtin_shufflevector(lo, hi4, 0, 1, 2, 3, 4, 5, 6, 7);
    }
  bf16x8 kf[2][2];
  {
    const gu64* gp = (const gu64*)flags + (wv * 16) * 64 + lane;
    unsigned val[16];
    bool okall = false;
    for (int spin = 0; spin < 4000; ++spin) {
      bool ok = true;
#pragma unroll
      for (int i = 0; i < 16; ++i) {
        unsigned long long x = __hip_atomic_load(gp + i * 64, RLX_AGENT);
        val[i] = (unsigned)x;
        ok = ok && ((x >> 32) == 1ull);
      }
      if (__all(ok)) { okall = true; break; }
      __builtin_amdgcn_s_sleep(2);
    }
    typedef __attribute__((ext_vector_type(4))) unsigned u32x4;
    if (okall) {
#pragma unroll
      for (int q = 0; q < 4; ++q)
        kf[q >> 1][q & 1] = __builtin_bit_cast(bf16x8, u32x4{val[4 * q], val[4 * q + 1], val[4 * q + 2], val[4 * q + 3]});
    } else {
      bf16* mine = slow + (long)b * 16384;
#pragma unroll 1
      for (int i = 0; i < 8; ++i) {
        const int h = wv * 2 + (i >> 2), t4 = (i & 3) * 64 + lane;
        *(bf16x4*)(mine + h * 1024 + t4 * 4) = reduce_head(part, h, t4);
      }
      asm volatile("s_waitcnt vmcnt(0)" ::: "memory");
#pragma unroll
      for (int hi = 0; hi < 2; ++hi) {
        kf[hi][0] = *(const bf16x8*)(mine + (wv * 2 + hi) * 1024 + c16 * 32 + 8 * g);
        kf[hi][1] = *(const bf16x8*)(mine + (wv * 2 + hi) * 1024 + (16 + c16) * 32 + 8 * g);
      }
    }
  }
  STAMP(1,2);
#pragma unroll
  for (int hi = 0; hi < 2; ++hi) {
    const int h = wv * 2 + hi;
    f32x4 n0v[4], n1v[4];
#pragma unroll
    for (int t = 0; t < 4; ++t) {
      n0v[t] = mfma16(kf[hi][0], qf[hi][t], f32x4{0, 0, 0, 0});
      n1v[t] = mfma16(kf[hi][1], qf[hi][t], f32x4{0, 0, 0, 0});
    }
#pragma unroll
    for (int t = 0; t < 4; ++t) {
      const float inv = __builtin_amdgcn_rcpf(bcast_row3(n1v[t][0]) + 1e-6f);
      bf16x4 o0, o1;
      f32x4 r0 = n0v[t] * inv, r1 = n1v[t] * inv;
#pragma unroll
      for (int j = 0; j < 4; ++j) { o0[j] = (__bf16)r0[j]; o1[j] = (__bf16)r1[j]; }
      const int row = 16 * t + c16;
      const int col = h * 28 + 4 * g;
      *(bf16x4*)(lds + (col >> 6) * 8192 + row * 128 + ((((col & 63) >> 3) ^ (row & 7)) << 4) + (col & 7) * 2) = o0;
      const int col1 = col + 16;
      int a1 = (col1 >> 6) * 8192 + row * 128 + ((((col1 & 63) >> 3) ^ (row & 7)) << 4) + (col1 & 7) * 2;
      a1 = (g < 3) ? a1 : OP_LDS + 16 + lane * 8;
      *(bf16x4*)(lds + a1) = o1;
    }
  }
  }
  asm volatile("s_waitcnt vmcnt(0)" ::: "memory");
  BARRIER();
  STAMP(1,3);
  if (wv < 6) op_compute<2>(lds, out, m0, br * DM + n0, (wv >> 1) * 2, (wv & 1) * 32, lane);
  else        op_compute<1>(lds, out, m0, br * DM + n0, 6, (wv & 1) * 32, lane);
  STAMP(1,4);
}

extern "C" void kernel_launch(void* const* d_in, const int* in_sizes, int n_in,
                              void* d_out, int out_size, void* d_ws, size_t ws_size,
                              hipStream_t stream) {
  const bf16* xa = (const bf16*)d_in[0];
  const bf16* wqa = (const bf16*)d_in[1];
  const bf16* woa = (const bf16*)d_in[2];
  const bf16* xb = (const bf16*)d_in[3];
  const bf16* wqb = (const bf16*)d_in[4];
  const bf16* wob = (const bf16*)d_in[5];
  bf16* out = (bf16*)d_out;
  char* ws = (char*)d_ws;
  bf16* pq = (bf16*)ws;
  bf16* ob = (bf16*)(ws + 2 * 1024 * 1024);
  float* part = (float*)(ws + 4 * 1024 * 1024);
  bf16* kvt = (bf16*)(ws + 6 * 1024 * 1024);
  unsigned* flags = (unsigned*)(ws + 7 * 1024 * 1024);
  bf16* dummy = (bf16*)(ws + 7 * 1024 * 1024 + 131072);
  bf16* slow = (bf16*)(ws + 8 * 1024 * 1024);

  (void)hipFuncSetAttribute((const void*)k1_qkv, hipFuncAttributeMaxDynamicSharedMemorySize, K1_LDS);
  (void)hipFuncSetAttribute((const void*)k2_out, hipFuncAttributeMaxDynamicSharedMemorySize, OP_LDS + 1024);

  k1_qkv<<<256, 768, K1_LDS, stream>>>(xa, wqa, xb, wqb, pq, ob, part, dummy, flags);
  k2_out<<<256, 512, OP_LDS + 1024, stream>>>(pq, woa, ob, wob, part, kvt, flags, slow, out);
}
```

```cpp
#include <hip/hip_runtime.h>
#include <hip/hip_bf16.h>
#include <stdint.h>

typedef __bf16 bf16;
typedef __attribute__((ext_vector_type(8))) __bf16 bf16x8;
typedef __attribute__((ext_vector_type(4))) __bf16 bf16x4;
typedef __attribute__((ext_vector_type(4))) float f32x4;
#define LAS __attribute__((address_space(3)))
#define DI __device__ __forceinline__

#define STAMP(k,i) do {} while (0)
constexpr int SEQ = 2048, DM = 448, NQKV = 1344;

DI float bfr(float x) { return (float)(__bf16)x; }

typedef __attribute__((ext_vector_type(2))) __bf16 bf16x2;
DI f32x4 bfr4(f32x4 v) {
  bf16x2 a = {(__bf16)v[0], (__bf16)v[1]}, b = {(__bf16)v[2], (__bf16)v[3]};
  unsigned ua = __builtin_bit_cast(unsigned, a), ub = __builtin_bit_cast(unsigned, b);
  asm volatile("" : "+v"(ua), "+v"(ub));
  return f32x4{__uint_as_float(ua << 16), __uint_as_float(ua & 0xffff0000u), __uint_as_float(ub << 16), __uint_as_float(ub & 0xffff0000u)};
}
DI void glds16(const void* gsrc, unsigned lds_dst) {
  unsigned keep;
  asm volatile("s_mov_b32 %0, m0\n\ts_mov_b32 m0, %2\n\ts_nop 0\n\tglobal_load_lds_dwordx4 %1, off\n\ts_mov_b32 m0, %0"
               : "=&s"(keep) : "v"(gsrc), "s"(lds_dst) : "memory");
}
#define WAIT_VM(n) asm volatile("s_waitcnt vmcnt(" #n ")" ::: "memory")
#define BARRIER() do { asm volatile("s_waitcnt lgkmcnt(0)" ::: "memory"); __builtin_amdgcn_s_barrier(); asm volatile("" ::: "memory"); } while (0)

DI bf16x8 tr_frag(const char* p, int hi_off) {
  bf16x4 lo = __builtin_amdgcn_ds_read_tr16_b64_v4bf16((LAS bf16x4*)(p));
  bf16x4 hi = __builtin_amdgcn_ds_read_tr16_b64_v4bf16((LAS bf16x4*)(p + hi_off));
  return __builtin_shufflevector(lo, hi, 0, 1, 2, 3, 4, 5, 6, 7);
}
DI f32x4 mfma16(bf16x8 a, bf16x8 b, f32x4 c) { return __builtin_amdgcn_mfma_f32_16x16x32_bf16(a, b, c, 0, 0, 0); }

DI bf16x8 pack8(f32x4 a, f32x4 b) {
  bf16x8 r;
#pragma unroll
  for (int i = 0; i < 4; ++i) { r[i] = (__bf16)a[i]; r[4 + i] = (__bf16)b[i]; }
  return r;
}
DI float phi_f(float t) {
  float r = t > 0.f ? t + 1.0f : bfr(__expf(t)) + 1e-6f;
  return bfr(r);
}
DI f32x4 phi4(f32x4 acc) {
  f32x4 t = bfr4(acc), e;
#pragma unroll
  for (int j = 0; j < 4; ++j) e[j] = t[j] > 0.f ? t[j] + 1.0f : __expf(t[j]) + 1e-6f;
  return e;
}
DI float xmax16_32(float m) {
  unsigned u = __float_as_uint(m);
  auto r = __builtin_amdgcn_permlane16_swap(u, u, false, false);
  float a = fmaxf(__uint_as_float(r[0]), __uint_as_float(r[1]));
  unsigned v = __float_as_uint(a);
  auto r2 = __builtin_amdgcn_permlane32_swap(v, v, false, false);
  return fmaxf(__uint_as_float(r2[0]), __uint_as_float(r2[1]));
}
DI float xsum16_32(float m) {
  unsigned u = __float_as_uint(m);
  auto r = __builtin_amdgcn_permlane16_swap(u, u, false, false);
  float a = __uint_as_float(r[0]) + __uint_as_float(r[1]);
  unsigned v = __float_as_uint(a);
  auto r2 = __builtin_amdgcn_permlane32_swap(v, v, false, false);
  return __uint_as_float(r2[0]) + __uint_as_float(r2[1]);
}
DI float bcast_row3(float x) {
  unsigned u = __float_as_uint(x);
  auto r = __builtin_amdgcn_permlane32_swap(u, u, false, false);
  unsigned v = r[1];
  auto r2 = __builtin_amdgcn_permlane16_swap(v, v, false, false);
  return __uint_as_float(r2[1]);
}
template <int NM, int ND> DI void interleave() {
  constexpr int LEAD = ND > NM ? ND - NM : 0;
  if (LEAD) __builtin_amdgcn_sched_group_barrier(0x100, LEAD, 0);
#pragma unroll
  for (int i = 0; i < NM; ++i) {
    __builtin_amdgcn_sched_group_barrier(0x008, 1, 0);
    if (i + LEAD < ND) __builtin_amdgcn_sched_group_barrier(0x100, 1, 0);
  }
}
DI int pi_inv(int lr) { return lr < 16 ? 8 * (lr >> 2) + (lr & 3) : 8 * ((lr - 16) >> 2) + 4 + (lr & 3); }

constexpr int K1_XB = 144 * 128;
constexpr int K1_WB = 64 * 352;
constexpr int K1_STAGE = K1_XB + K1_WB;
constexpr int K1_LDS = 3 * K1_STAGE;

DI void k1_loader(const bf16* __restrict__ x, const bf16* __restrict__ w, int rt, int hg, char* lds, int wv, int lane) {
  const int s0 = rt * 128;
  const unsigned ldsbase = (unsigned)(size_t)lds;
  const char* src[10];
  unsigned dst[10];
  long strd0;
#pragma unroll
  for (int i = 0; i < 4; ++i) {
    int xi = 2 + wv + 4 * i;
    int row = xi * 8 + (lane >> 3), pos = lane & 7;
    int grow = s0 - 16 + row;
    src[i] = (const char*)(x + (long)grow * DM + ((pos ^ (row & 7)) * 8));
    dst[i] = xi * 1024;
  }
#pragma unroll
  for (int i = 0; i < 6; ++i) {
    int j = wv + 4 * i;
    if (i == 0 && j < 2) {
      int row = j * 8 + (lane >> 3), pos = lane & 7;
      int grow = s0 - 16 + row; grow = grow < 0 ? 0 : grow;
      src[4] = (const char*)(x + (long)grow * DM + ((pos ^ (row & 7)) * 8));
      dst[4] = j * 1024;
      strd0 = 128;
    } else {
      int wi = j - 2;
      int c = wi * 64 + lane;
      int lrow = c / 22, pos = c - lrow * 22;
      pos = pos > 20 ? 20 : pos;
      int k = (lrow & 32) + pi_inv(lrow & 31);
      int seg = pos / 7, cc = pos - seg * 7;
      src[4 + i] = (const char*)(w + (long)k * NQKV + seg * DM + hg * 56 + cc * 8);
      dst[4 + i] = K1_XB + wi * 1024;
      if (i == 0) strd0 = 64L * NQKV * 2;
    }
  }
  auto issue = [&](int st, int buf) {
    unsigned b = ldsbase + buf * K1_STAGE;
#pragma unroll
    for (int i = 0; i < 4; ++i) glds16(src[i] + st * 128, b + dst[i]);
    glds16(src[4] + st * strd0, b + dst[4]);
#pragma unroll
    for (int i = 5; i < 10; ++i) glds16(src[i] + (long)st * (64L * NQKV * 2), b + dst[i]);
  };
  issue(0, 0);
  issue(1, 1);
#pragma unroll 1
  for (int st = 0; st < 7; ++st) {
    if (st < 6) WAIT_VM(10); else WAIT_VM(0);
    __builtin_amdgcn_s_barrier();
    if (st + 2 < 7) issue(st + 2, (st + 2) % 3);
  }
}

template <int BR>
DI void k1_body(bf16* __restrict__ outq, float* __restrict__ part, bf16* __restrict__ dummy, int rt, int hg, char* lds,
                int wv, int lane) {
  const int rq = wv >> 1, hh = wv & 1;
  const int g = lane >> 4, c16 = lane & 15;
  const int s0 = rt * 128;

  constexpr int T0 = BR ? 0 : 1;
  f32x4 aq[2][2], ak[3][2], av[3][2];
#pragma unroll
  for (int t = 0; t < 3; ++t)
#pragma unroll
    for (int c = 0; c < 2; ++c) {
      if (t < 2) aq[t][c] = f32x4{0, 0, 0, 0};
      ak[t][c] = f32x4{0, 0, 0, 0}; av[t][c] = f32x4{0, 0, 0, 0};
    }

  STAMP(0,0);
  const int xoff = (rq * 32 + c16) * 128;
  const int woff = K1_XB + (4 * g + (c16 >> 2)) * 352 + hh * 56 + (c16 & 3) * 8;
  struct Frags { bf16x8 xf[3], wf[6]; };
  auto ldf = [&](Frags& f, const char* sb, int ks) {
    const int xs = ((ks * 4 + g) ^ (c16 & 7)) << 4;
#pragma unroll
    for (int ct = 0; ct < 6; ++ct)
      f.wf[ct] = tr_frag(sb + woff + ks * 32 * 352 + (ct >> 1) * 112 + (ct & 1) * 32, 16 * 352);
#pragma unroll
    for (int t = T0; t < 3; ++t) f.xf[t] = *(const bf16x8*)(sb + xoff + t * 2048 + xs);
  };
  auto mma = [&](const Frags& f) {
#pragma unroll
    for (int t = T0; t < 3; ++t) {
#pragma unroll
      for (int c = 0; c < 2; ++c) {
        if (t >= 1) aq[t - 1][c] = mfma16(f.wf[c], f.xf[t], aq[t - 1][c]);
        if (BR) ak[t][c] = mfma16(f.wf[2 + c], f.xf[t], ak[t][c]);
        else    ak[t][c] = mfma16(f.xf[t], f.wf[2 + c], ak[t][c]);
        av[t][c] = mfma16(f.xf[t], f.wf[4 + c], av[t][c]);
      }
    }
  };
  Frags fa, fb;
  BARRIER();
  STAMP(0,2);
  ldf(fa, lds, 0);
  int buf = 0;
#pragma unroll 1
  for (int st = 0; st < 6; ++st) {
    if (st == 3) STAMP(0,3);
    ldf(fb, lds + buf * K1_STAGE, 1);
    mma(fa);
    interleave<BR ? 16 : 12, BR ? 15 : 14>();
    __builtin_amdgcn_sched_barrier(0);
    buf = buf == 2 ? 0 : buf + 1;
    BARRIER();
    ldf(fa, lds + buf * K1_STAGE, 0);
    mma(fb);
    interleave<BR ? 16 : 12, BR ? 15 : 14>();
    __builtin_amdgcn_sched_barrier(0);
  }
  ldf(fb, lds + buf * K1_STAGE, 1);
  mma(fa);
  interleave<BR ? 16 : 12, BR ? 15 : 14>();
  __builtin_amdgcn_sched_barrier(0);
  mma(fb);

  STAMP(0,4);
  const int h = hg * 2 + hh;
  const int srow = s0 + rq * 32 + c16;
  if (BR) {
    const float scale = 0.18898223650461362f;
    bf16x8 kp[3], qp[2];
#pragma unroll
    for (int t = 0; t < 3; ++t) {
      f32x4 hi = ak[t][1];
      if (g == 3) hi = f32x4{0, 0, 0, 0};
      kp[t] = pack8(ak[t][0], hi);
    }
#pragma unroll
    for (int t = 0; t < 2; ++t) {
      f32x4 hi = aq[t][1];
      if (g == 3) hi = f32x4{0, 0, 0, 0};
      qp[t] = pack8(aq[t][0], hi);
    }
    f32x4 sp[2], sc[2];
#pragma unroll
    for (int t = 0; t < 2; ++t) {
      sp[t] = mfma16(kp[t], qp[t], f32x4{0, 0, 0, 0});
      sc[t] = mfma16(kp[t + 1], qp[t], f32x4{0, 0, 0, 0});
    }
    bool okp[4], okc[4];
#pragma unroll
    for (int j = 0; j < 4; ++j) {
      int dp = c16 + 16 - 4 * g - j, dc = c16 - 4 * g - j;
      okp[j] = ((dp & 1) == 0) && dp <= 14;
      okc[j] = dc >= 0 && ((dc & 1) == 0);
    }
    const bool first = (s0 + rq * 32) == 0;
    const float NEG = -__builtin_inff();
    float m[2];
#pragma unroll
    for (int t = 0; t < 2; ++t) {
#pragma unroll
      for (int j = 0; j < 4; ++j) {
        bool op = okp[j] && !(t == 0 && first);
        sp[t][j] = op ? sp[t][j] : NEG;
        sc[t][j] = okc[j] ? sc[t][j] : NEG;
      }
      m[t] = fmaxf(fmaxf(fmaxf(sp[t][0], sp[t][1]), fmaxf(sp[t][2], sp[t][3])),
                   fmaxf(fmaxf(sc[t][0], sc[t][1]), fmaxf(sc[t][2], sc[t][3])));
    }
#pragma unroll
    for (int t = 0; t < 2; ++t) m[t] = xmax16_32(m[t]);
    const float c2 = scale * 1.4426950408889634f;
    float sum[2];
#pragma unroll
    for (int t = 0; t < 2; ++t) {
      const float mc = -m[t] * c2;
#pragma unroll
      for (int j = 0; j < 4; ++j) {
        sp[t][j] = __builtin_amdgcn_exp2f(fmaf(sp[t][j], c2, mc));
        sc[t][j] = __builtin_amdgcn_exp2f(fmaf(sc[t][j], c2, mc));
      }
      f32x4 sv = sp[t] + sc[t];
      sum[t] = (sv[0] + sv[1]) + (sv[2] + sv[3]);
    }
#pragma unroll
    for (int t = 0; t < 2; ++t) sum[t] = __builtin_amdgcn_rcpf(xsum16_32(sum[t]));
    f32x4 o[2][2];
#pragma unroll
    for (int t = 0; t < 2; ++t) {
      bf16x8 pp = pack8(sp[t] * sum[t], sc[t] * sum[t]);
#pragma unroll
      for (int et = 0; et < 2; ++et) o[t][et] = mfma16(pack8(av[t][et], av[t + 1][et]), pp, f32x4{0, 0, 0, 0});
    }
#pragma unroll
    for (int t = 0; t < 2; ++t)
#pragma unroll
      for (int et = 0; et < 2; ++et) {
        bf16x4 ob;
#pragma unroll
        for (int j = 0; j < 4; ++j) ob[j] = (__bf16)o[t][et][j];
        bf16* dp = outq + (long)(srow + 16 * t) * DM + h * 28 + 16 * et + 4 * g;
        if (et == 1) dp = (g < 3) ? dp : dummy + lane * 4;
        *(bf16x4*)dp = ob;
      }
  } else {
#pragma unroll
    for (int t = 0; t < 2; ++t)
#pragma unroll
      for (int c = 0; c < 2; ++c) {
        bf16x4 ob;
        f32x4 pv = phi4(aq[t][c]);
#pragma unroll
        for (int j = 0; j < 4; ++j) ob[j] = (__bf16)pv[j];
        bf16* dp = outq + (long)(srow + 16 * t) * DM + h * 28 + 16 * c + 4 * g;
        if (c == 1) dp = (g < 3) ? dp : dummy + lane * 4;
        *(bf16x4*)dp = ob;
      }
#pragma unroll
    for (int t = 1; t < 3; ++t) {
      ak[t][0] = phi4(ak[t][0]);
      ak[t][1] = phi4(ak[t][1]);
#pragma unroll
      for (int j = 0; j < 4; ++j)
        if (c16 >= 12) av[t][1][j] = (c16 == 12) ? 1.0f : 0.0f;
    }
    f32x4 kv[2][2];
#pragma unroll
    for (int dc = 0; dc < 2; ++dc)
#pragma unroll
      for (int ec = 0; ec < 2; ++ec)
        kv[dc][ec] = mfma16(pack8(ak[1][dc], ak[2][dc]), pack8(av[1][ec], av[2][ec]), f32x4{0, 0, 0, 0});
    BARRIER();
    f32x4* ex = (f32x4*)(lds) + hh * 1024 + lane;
    if (rq != 0) {
#pragma unroll
      for (int i = 0; i < 4; ++i) ex[(rq * 4 + i) * 64] = kv[i >> 1][i & 1];
    }
    BARRIER();
    if (rq == 0) {
      float* pb = part + ((long)rt * 16 + h) * 1024;
#pragma unroll
      for (int i = 0; i < 4; ++i) {
        f32x4 o = ((kv[i >> 1][i & 1] + ex[(4 + i) * 64]) + ex[(8 + i) * 64]) + ex[(12 + i) * 64];
        *(f32x4*)(pb + (16 * (i & 1) + c16) * 32 + 16 * (i >> 1) + 4 * g) = o;
      }
    }
  }
}

__global__ __launch_bounds__(768, 3) void k1_qkv(const bf16* __restrict__ xa, const bf16* __restrict__ wa,
                                                 const bf16* __restrict__ xb, const bf16* __restrict__ wb,
                                                 bf16* __restrict__ pq, bf16* __restrict__ ob,
                                                 float* __restrict__ part, bf16* __restrict__ dummy,
                                                 unsigned* __restrict__ flags,
                                                 const bf16* __restrict__ woa, const bf16* __restrict__ wob) {
  extern __shared__ __attribute__((aligned(16))) char lds[];
  const int b = blockIdx.x;
  const int lane = threadIdx.x & 63;
  const int wv = __builtin_amdgcn_readfirstlane(threadIdx.x >> 6);
  const int xcd = b & 7, idx = b >> 3;
  const int br = xcd & 1, sub = xcd >> 1;
  const int rt = (sub >> 1) * 8 + (idx >> 2), hg = (sub & 1) * 4 + (idx & 3);
  if (wv >= 8) {
    if (wv == 8) flags[b * 64 + lane] = 0u;
    k1_loader(br ? xb : xa, br ? wb : wa, rt, hg, lds, wv - 8, lane);
    const int line = idx * 98 + (wv - 8) * 64 + lane;
    if (wv < 10 && line < (idx + 1) * 98) {
      unsigned v = *(const unsigned*)((const char*)(br ? wob : woa) + line * 128);
      asm volatile("" :: "v"(v));
    }
    return;
  }
  if (br == 0) k1_body<0>(pq, part, dummy, rt, hg, lds, wv, lane);
  else         k1_body<1>(ob, part, dummy, rt, hg, lds, wv, lane);
  STAMP(0,5);
}

constexpr int OP_OB = 7 * 64 * 128;
constexpr int OP_WB = 448 * 224;
constexpr int OP_LDS = OP_OB + OP_WB;

DI void op_issue_a(const bf16* __restrict__ a, int m0, int wv, int lane, unsigned ldsbase) {
  const int row = wv * 8 + (lane >> 3), pos = lane & 7;
  const int aoff = ((m0 + row) * DM + ((pos ^ (row & 7)) * 8)) * 2;
#pragma unroll
  for (int kc = 0; kc < 7; ++kc) glds16((const char*)a + aoff + kc * 128, ldsbase + kc * 8192 + wv * 1024);
}
template <int NW>
DI void op_issue_w(const bf16* __restrict__ wo, int n0, int wv, int lane, unsigned ldsbase) {
  int woffs[NW];
#pragma unroll
  for (int i = 0; i < NW; ++i) {
    int c = (wv + 8 * i) * 64 + lane;
    int lrow = c / 14, pos = c - lrow * 14;
    woffs[i] = (((lrow & 32) + pi_inv(lrow & 31)) * DM + n0 + pos * 8) * 2;
  }
#pragma unroll
  for (int kc = 0; kc < 7; ++kc)
#pragma unroll
    for (int i = 0; i < NW; ++i)
      glds16((const char*)wo + woffs[i] + kc * (64 * DM * 2), ldsbase + OP_OB + kc * 14336 + (wv + 8 * i) * 1024);
}

template <int NC>
DI void op_compute(const char* lds, bf16* __restrict__ out, int m0, int ncol0, int ct0, int rbase, int lane) {
  const int g = lane >> 4, c16 = lane & 15;
  f32x4 acc[2][NC];
#pragma unroll
  for (int r = 0; r < 2; ++r)
#pragma unroll
    for (int c = 0; c < NC; ++c) acc[r][c] = f32x4{0, 0, 0, 0};
  const char* op = lds + (rbase + c16) * 128;
  const char* wp = lds + OP_OB + (4 * g + (c16 >> 2)) * 224 + ct0 * 32 + (c16 & 3) * 8;
  const int xs0 = (g ^ (c16 & 7)) << 4, xs1 = ((4 + g) ^ (c16 & 7)) << 4;
  struct Fr { bf16x8 of[2], wf[NC]; };
  auto ldf = [&](Fr& f, int xs, int wo) {
#pragma unroll
    for (int c = 0; c < NC; ++c) f.wf[c] = tr_frag(wp + wo + c * 32, 16 * 224);
#pragma unroll
    for (int r = 0; r < 2; ++r) f.of[r] = *(const bf16x8*)(op + r * 2048 + xs);
  };
  auto mma = [&](const Fr& f) {
#pragma unroll
    for (int r = 0; r < 2; ++r)
#pragma unroll
      for (int c = 0; c < NC; ++c) acc[r][c] = mfma16(f.wf[c], f.of[r], acc[r][c]);
  };
  Fr fa, fb;
  ldf(fa, xs0, 0);
#pragma unroll 1
  for (int kc = 0; kc < 6; ++kc) {
    ldf(fb, xs1, 32 * 224);
    mma(fa);
    __builtin_amdgcn_sched_barrier(0);
    op += 8192; wp += 64 * 224;
    ldf(fa, xs0, 0);
    mma(fb);
    __builtin_amdgcn_sched_barrier(0);
  }
  ldf(fb, xs1, 32 * 224);
  mma(fa);
  mma(fb);
#pragma unroll
  for (int r = 0; r < 2; ++r)
#pragma unroll
    for (int c = 0; c < NC; ++c) {
      bf16x4 ob;
#pragma unroll
      for (int j = 0; j < 4; ++j) ob[j] = (__bf16)acc[r][c][j];
      *(bf16x4*)(out + (long)(m0 + rbase + r * 16 + c16) * (2 * DM) + ncol0 + (ct0 + c) * 16 + 4 * g) = ob;
    }
}

#define RLX_AGENT __ATOMIC_RELAXED, __HIP_MEMORY_SCOPE_AGENT
typedef __attribute__((address_space(1))) unsigned gu32;
typedef __attribute__((address_space(1))) unsigned long long gu64;

DI bf16x4 reduce_head(const float* __restrict__ part, int h, int tid) {
  const float* p = part + (long)h * 1024 + tid * 4;
  f32x4 v[16];
#pragma unroll
  for (int i = 0; i < 16; ++i) v[i] = *(const f32x4*)(p + (long)i * 16 * 1024);
  f32x4 s = v[0];
#pragma unroll
  for (int i = 1; i < 16; ++i) s += v[i];
  const int e = tid >> 3, d = (tid & 7) * 4;
  bf16x4 r;
#pragma unroll
  for (int j = 0; j < 4; ++j) r[j] = (__bf16)((e <= 28 && d + j < 28) ? s[j] : 0.f);
  return r;
}

__global__ __launch_bounds__(512, 2) void k2_out(const bf16* __restrict__ pq, const bf16* __restrict__ woa,
                                                 const bf16* __restrict__ ob, const bf16* __restrict__ wob,
                                                 const float* __restrict__ part, bf16* kvt,
                                                 unsigned* flags, bf16* slow, bf16* __restrict__ out) {
  extern __shared__ __attribute__((aligned(16))) char lds[];
  const int tid = threadIdx.x, lane = tid & 63;
  const int wv = __builtin_amdgcn_readfirstlane(tid >> 6);
  const int g = lane >> 4, c16 = lane & 15;
  const int b = blockIdx.x;
  const int xcd = b & 7, idx = b >> 3;
  const int br = xcd & 1, sub = xcd >> 1;
  const int mt = sub * 8 + (idx >> 2), nt = idx & 3;
  const int m0 = mt * 64, n0 = nt * 112;
  const unsigned ldsbase = (unsigned)(size_t)lds;
  STAMP(1,0);
  if (br == 1) {
    if (idx < 4 && wv < 4) {
      const int h = sub * 4 + idx;
      bf16x4 r = reduce_head(part, h, tid);
      const int e = tid >> 3, d = (tid & 7) * 4;
      const unsigned long long v = __builtin_bit_cast(unsigned long long, r);
      gu64* gp = (gu64*)flags + (((h * 2 + (e >> 4)) * 4 + ((d & 7) >> 1)) * 64 + (d >> 3) * 16 + (e & 15));
      __hip_atomic_store(gp, (1ull << 32) | (v & 0xffffffffull), RLX_AGENT);
      __hip_atomic_store(gp + 64, (1ull << 32) | (v >> 32), RLX_AGENT);
    }
    op_issue_a(ob, m0, wv, lane, ldsbase);
    if (wv < 6) op_issue_w<2>(wob, n0, wv, lane, ldsbase);
    else        op_issue_w<1>(wob, n0, wv, lane, ldsbase);
    STAMP(1,1);
  } else {
  op_issue_a(pq, m0, wv, lane, ldsbase);
  WAIT_VM(0);
  BARRIER();
  STAMP(1,1);
  bf16x8 qf[2][4];
#pragma unroll
  for (int hi = 0; hi < 2; ++hi)
#pragma unroll
    for (int t = 0; t < 4; ++t) {
      const int row = 16 * t + c16;
      const int col = (wv * 2 + hi) * 28 + 8 * g, col1 = col + 4;
      bf16x4 lo = *(const bf16x4*)(lds + (col >> 6) * 8192 + row * 128 + ((((col & 63) >> 3) ^ (row & 7)) << 4) + (col & 7) * 2);
      int a1 = (col1 >> 6) * 8192 + row * 128 + ((((col1 & 63) >> 3) ^ (row & 7)) << 4) + (col1 & 7) * 2;
      bf16x4 hi4 = *(const bf16x4*)(lds + a1);
      if (g == 3) { hi4[0] = (__bf16)0.f; hi4[1] = (__bf16)0.f; hi4[2] = (__bf16)0.f; hi4[3] = (__bf16)0.f; }
      qf[hi][t] = __builtin_shufflevector(lo, hi4, 0, 1, 2, 3, 4, 5, 6, 7);
    }
  bf16x8 kf[2][2];
  {
    const gu64* gp = (const gu64*)flags + (wv * 16) * 64 + lane;
    unsigned val[16];
    bool okall = false;
    for (int spin = 0; spin < 4000; ++spin) {
      bool ok = true;
#pragma unroll
      for (int i = 0; i < 16; ++i) {
        unsigned long long x = __hip_atomic_load(gp + i * 64, RLX_AGENT);
        val[i] = (unsigned)x;
        ok = ok && ((x >> 32) == 1ull);
      }
      if (__all(ok)) { okall = true; break; }
      __builtin_amdgcn_s_sleep(2);
    }
    typedef __attribute__((ext_vector_type(4))) unsigned u32x4;
    if (okall) {
#pragma unroll
      for (int q = 0; q < 4; ++q)
        kf[q >> 1][q & 1] = __builtin_bit_cast(bf16x8, u32x4{val[4 * q], val[4 * q + 1], val[4 * q + 2], val[4 * q + 3]});
    } else {
      bf16* mine = slow + (long)b * 16384;
#pragma unroll 1
      for (int i = 0; i < 8; ++i) {
        const int h = wv * 2 + (i >> 2), t4 = (i & 3) * 64 + lane;
        *(bf16x4*)(mine + h * 1024 + t4 * 4) = reduce_head(part, h, t4);
      }
      asm volatile("s_waitcnt vmcnt(0)" ::: "memory");
#pragma unroll
      for (int hi = 0; hi < 2; ++hi) {
        kf[hi][0] = *(const bf16x8*)(mine + (wv * 2 + hi) * 1024 + c16 * 32 + 8 * g);
        kf[hi][1] = *(const bf16x8*)(mine + (wv * 2 + hi) * 1024 + (16 + c16) * 32 + 8 * g);
      }
    }
  }
  STAMP(1,2);
  if (wv < 6) op_issue_w<2>(woa, n0, wv, lane, ldsbase);
  else        op_issue_w<1>(woa, n0, wv, lane, ldsbase);
#pragma unroll
  for (int hi = 0; hi < 2; ++hi) {
    const int h = wv * 2 + hi;
    f32x4 n0v[4], n1v[4];
#pragma unroll
    for (int t = 0; t < 4; ++t) {
      n0v[t] = mfma16(kf[hi][0], qf[hi][t], f32x4{0, 0, 0, 0});
      n1v[t] = mfma16(kf[hi][1], qf[hi][t], f32x4{0, 0, 0, 0});
    }
#pragma unroll
    for (int t = 0; t < 4; ++t) {
      const float inv = __builtin_amdgcn_rcpf(bcast_row3(n1v[t][0]) + 1e-6f);
      bf16x4 o0, o1;
      f32x4 r0 = n0v[t] * inv, r1 = n1v[t] * inv;
#pragma unroll
      for (int j = 0; j < 4; ++j) { o0[j] = (__bf16)r0[j]; o1[j] = (__bf16)r1[j]; }
      const int row = 16 * t + c16;
      const int col = h * 28 + 4 * g;
      *(bf16x4*)(lds + (col >> 6) * 8192 + row * 128 + ((((col & 63) >> 3) ^ (row & 7)) << 4) + (col & 7) * 2) = o0;
      const int col1 = col + 16;
      int a1 = (col1 >> 6) * 8192 + row * 128 + ((((col1 & 63) >> 3) ^ (row & 7)) << 4) + (col1 & 7) * 2;
      a1 = (g < 3) ? a1 : OP_LDS + 16 + lane * 8;
      *(bf16x4*)(lds + a1) = o1;
    }
  }
  }
  asm volatile("s_waitcnt vmcnt(0)" ::: "memory");
  BARRIER();
  STAMP(1,3);
  if (wv < 6) op_compute<2>(lds, out, m0, br * DM + n0, (wv >> 1) * 2, (wv & 1) * 32, lane);
  else        op_compute<1>(lds, out, m0, br * DM + n0, 6, (wv & 1) * 32, lane);
  STAMP(1,4);
}

extern "C" void kernel_launch(void* const* d_in, const int* in_sizes, int n_in,
                              void* d_out, int out_size, void* d_ws, size_t ws_size,
                              hipStream_t stream) {
  const bf16* xa = (const bf16*)d_in[0];
  const bf16* wqa = (const bf16*)d_in[1];
  const bf16* woa = (const bf16*)d_in[2];
  const bf16* xb = (const bf16*)d_in[3];
  const bf16* wqb = (const bf16*)d_in[4];
  const bf16* wob = (const bf16*)d_in[5];
  bf16* out = (bf16*)d_out;
  char* ws = (char*)d_ws;
  bf16* pq = (bf16*)ws;
  bf16* ob = (bf16*)(ws + 2 * 1024 * 1024);
  float* part = (float*)(ws + 4 * 1024 * 1024);
  bf16* kvt = (bf16*)(ws + 6 * 1024 * 1024);
  unsigned* flags = (unsigned*)(ws + 7 * 1024 * 1024);
  bf16* dummy = (bf16*)(ws + 7 * 1024 * 1024 + 131072);
  bf16* slow = (bf16*)(ws + 8 * 1024 * 1024);

  (void)hipFuncSetAttribute((const void*)k1_qkv, hipFuncAttributeMaxDynamicSharedMemorySize, K1_LDS);
  (void)hipFuncSetAttribute((const void*)k2_out, hipFuncAttributeMaxDynamicSharedMemorySize, OP_LDS + 1024);

  k1_qkv<<<256, 768, K1_LDS, stream>>>(xa, wqa, xb, wqb, pq, ob, part, dummy, flags, woa, wob);
  k2_out<<<256, 512, OP_LDS + 1024, stream>>>(pq, woa, ob, wob, part, kvt, flags, slow, out);
}
```

```cpp
#include <hip/hip_runtime.h>
#include <hip/hip_bf16.h>
#include <stdint.h>

typedef __bf16 bf16;
typedef __attribute__((ext_vector_type(8))) __bf16 bf16x8;
typedef __attribute__((ext_vector_type(4))) __bf16 bf16x4;
typedef __attribute__((ext_vector_type(4))) float f32x4;
#define LAS __attribute__((address_space(3)))
#define DI __device__ __forceinline__

#define STAMP(k,i) do {} while (0)
constexpr int SEQ = 2048, DM = 448, NQKV = 1344;

DI float bfr(float x) { return (float)(__bf16)x; }

typedef __attribute__((ext_vector_type(2))) __bf16 bf16x2;
DI f32x4 bfr4(f32x4 v) {
  bf16x2 a = {(__bf16)v[0], (__bf16)v[1]}, b = {(__bf16)v[2], (__bf16)v[3]};
  unsigned ua = __builtin_bit_cast(unsigned, a), ub = __builtin_bit_cast(unsigned, b);
  asm volatile("" : "+v"(ua), "+v"(ub));
  return f32x4{__uint_as_float(ua << 16), __uint_as_float(ua & 0xffff0000u), __uint_as_float(ub << 16), __uint_as_float(ub & 0xffff0000u)};
}
DI void glds16(const void* gsrc, unsigned lds_dst) {
  unsigned keep;
  asm volatile("s_mov_b32 %0, m0\n\ts_mov_b32 m0, %2\n\ts_nop 0\n\tglobal_load_lds_dwordx4 %1, off\n\ts_mov_b32 m0, %0"
               : "=&s"(keep) : "v"(gsrc), "s"(lds_dst) : "memory");
}
#define WAIT_VM(n) asm volatile("s_waitcnt vmcnt(" #n ")" ::: "memory")
#define BARRIER() do { asm volatile("s_waitcnt lgkmcnt(0)" ::: "memory"); __builtin_amdgcn_s_barrier(); asm volatile("" ::: "memory"); } while (0)

DI bf16x8 tr_frag(const char* p, int hi_off) {
  bf16x4 lo = __builtin_amdgcn_ds_read_tr16_b64_v4bf16((LAS bf16x4*)(p));
  bf16x4 hi = __builtin_amdgcn_ds_read_tr16_b64_v4bf16((LAS bf16x4*)(p + hi_off));
  return __builtin_shufflevector(lo, hi, 0, 1, 2, 3, 4, 5, 6, 7);
}
DI f32x4 mfma16(bf16x8 a, bf16x8 b, f32x4 c) { return __builtin_amdgcn_mfma_f32_16x16x32_bf16(a, b, c, 0, 0, 0); }

DI bf16x8 pack8(f32x4 a, f32x4 b) {
  bf16x8 r;
#pragma unroll
  for (int i = 0; i < 4; ++i) { r[i] = (__bf16)a[i]; r[4 + i] = (__bf16)b[i]; }
  return r;
}
DI float phi_f(float t) {
  float r = t > 0.f ? t + 1.0f : bfr(__expf(t)) + 1e-6f;
  return bfr(r);
}
DI f32x4 phi4(f32x4 acc) {
  f32x4 t = bfr4(acc), e;
#pragma unroll
  for (int j = 0; j < 4; ++j) e[j] = t[j] > 0.f ? t[j] + 1.0f : __expf(t[j]) + 1e-6f;
  return e;
}
DI float xmax16_32(float m) {
  unsigned u = __float_as_uint(m);
  auto r = __builtin_amdgcn_permlane16_swap(u, u, false, false);
  float a = fmaxf(__uint_as_float(r[0]), __uint_as_float(r[1]));
  unsigned v = __float_as_uint(a);
  auto r2 = __builtin_amdgcn_permlane32_swap(v, v, false, false);
  return fmaxf(__uint_as_float(r2[0]), __uint_as_float(r2[1]));
}
DI float xsum16_32(float m) {
  unsigned u = __float_as_uint(m);
  auto r = __builtin_amdgcn_permlane16_swap(u, u, false, false);
  float a = __uint_as_float(r[0]) + __uint_as_float(r[1]);
  unsigned v = __float_as_uint(a);
  auto r2 = __builtin_amdgcn_permlane32_swap(v, v, false, false);
  return __uint_as_float(r2[0]) + __uint_as_float(r2[1]);
}
DI float bcast_row3(float x) {
  unsigned u = __float_as_uint(x);
  auto r = __builtin_amdgcn_permlane32_swap(u, u, false, false);
  unsigned v = r[1];
  auto r2 = __builtin_amdgcn_permlane16_swap(v, v, false, false);
  return __uint_as_float(r2[1]);
}
template <int NM, int ND> DI void interleave() {
  constexpr int LEAD = ND > NM ? ND - NM : 0;
  if (LEAD) __builtin_amdgcn_sched_group_barrier(0x100, LEAD, 0);
#pragma unroll
  for (int i = 0; i < NM; ++i) {
    __builtin_amdgcn_sched_group_barrier(0x008, 1, 0);
    if (i + LEAD < ND) __builtin_amdgcn_sched_group_barrier(0x100, 1, 0);
  }
}
DI int pi_inv(int lr) { return lr < 16 ? 8 * (lr >> 2) + (lr & 3) : 8 * ((lr - 16) >> 2) + 4 + (lr & 3); }

constexpr int K1_XB = 144 * 128;
constexpr int K1_WB = 64 * 352;
constexpr int K1_STAGE = K1_XB + K1_WB;
constexpr int K1_LDS = 3 * K1_STAGE;

DI void k1_loader(const bf16* __restrict__ x, const bf16* __restrict__ w, int rt, int hg, char* lds, int wv, int lane) {
  const int s0 = rt * 128;
  const unsigned ldsbase = (unsigned)(size_t)lds;
  const char* src[10];
  unsigned dst[10];
  long strd0;
#pragma unroll
  for (int i = 0; i < 4; ++i) {
    int xi = 2 + wv + 4 * i;
    int row = xi * 8 + (lane >> 3), pos = lane & 7;
    int grow = s0 - 16 + row;
    src[i] = (const char*)(x + (long)grow * DM + ((pos ^ (row & 7)) * 8));
    dst[i] = xi * 1024;
  }
#pragma unroll
  for (int i = 0; i < 6; ++i) {
    int j = wv + 4 * i;
    if (i == 0 && j < 2) {
      int row = j * 8 + (lane >> 3), pos = lane & 7;
      int grow = s0 - 16 + row; grow = grow < 0 ? 0 : grow;
      src[4] = (const char*)(x + (long)grow * DM + ((pos ^ (row & 7)) * 8));
      dst[4] = j * 1024;
      strd0 = 128;
    } else {
      int wi = j - 2;
      int c = wi * 64 + lane;
      int lrow = c / 22, pos = c - lrow * 22;
      pos = pos > 20 ? 20 : pos;
      int k = (lrow & 32) + pi_inv(lrow & 31);
      int seg = pos / 7, cc = pos - seg * 7;
      src[4 + i] = (const char*)(w + (long)k * NQKV + seg * DM + hg * 56 + cc * 8);
      dst[4 + i] = K1_XB + wi * 1024;
      if (i == 0) strd0 = 64L * NQKV * 2;
    }
  }
  auto issue = [&](int st, int buf) {
    unsigned b = ldsbase + buf * K1_STAGE;
#pragma unroll
    for (int i = 0; i < 4; ++i) glds16(src[i] + st * 128, b + dst[i]);
    glds16(src[4] + st * strd0, b + dst[4]);
#pragma unroll
    for (int i = 5; i < 10; ++i) glds16(src[i] + (long)st * (64L * NQKV * 2), b + dst[i]);
  };
  issue(0, 0);
  issue(1, 1);
#pragma unroll 1
  for (int st = 0; st < 7; ++st) {
    if (st < 6) WAIT_VM(10); else WAIT_VM(0);
    __builtin_amdgcn_s_barrier();
    if (st + 2 < 7) issue(st + 2, (st + 2) % 3);
  }
}

template <int BR>
DI void k1_body(bf16* __restrict__ outq, float* __restrict__ part, bf16* __restrict__ dummy, int rt, int hg, char* lds,
                int wv, int lane) {
  const int rq = wv >> 1, hh = wv & 1;
  const int g = lane >> 4, c16 = lane & 15;
  const int s0 = rt * 128;

  constexpr int T0 = BR ? 0 : 1;
  f32x4 aq[2][2], ak[3][2], av[3][2];
#pragma unroll
  for (int t = 0; t < 3; ++t)
#pragma unroll
    for (int c = 0; c < 2; ++c) {
      if (t < 2) aq[t][c] = f32x4{0, 0, 0, 0};
      ak[t][c] = f32x4{0, 0, 0, 0}; av[t][c] = f32x4{0, 0, 0, 0};
    }

  STAMP(0,0);
  const int xoff = (rq * 32 + c16) * 128;
  const int woff = K1_XB + (4 * g + (c16 >> 2)) * 352 + hh * 56 + (c16 & 3) * 8;
  struct Frags { bf16x8 xf[3], wf[6]; };
  auto ldf = [&](Frags& f, const char* sb, int ks) {
    const int xs = ((ks * 4 + g) ^ (c16 & 7)) << 4;
#pragma unroll
    for (int ct = 0; ct < 6; ++ct)
      f.wf[ct] = tr_frag(sb + woff + ks * 32 * 352 + (ct >> 1) * 112 + (ct & 1) * 32, 16 * 352);
#pragma unroll
    for (int t = T0; t < 3; ++t) f.xf[t] = *(const bf16x8*)(sb + xoff + t * 2048 + xs);
  };
  auto mma = [&](const Frags& f) {
#pragma unroll
    for (int t = T0; t < 3; ++t) {
#pragma unroll
      for (int c = 0; c < 2; ++c) {
        if (t >= 1) aq[t - 1][c] = mfma16(f.wf[c], f.xf[t], aq[t - 1][c]);
        if (BR) ak[t][c] = mfma16(f.wf[2 + c], f.xf[t], ak[t][c]);
        else    ak[t][c] = mfma16(f.xf[t], f.wf[2 + c], ak[t][c]);
        av[t][c] = mfma16(f.xf[t], f.wf[4 + c], av[t][c]);
      }
    }
  };
  Frags fa, fb;
  BARRIER();
  STAMP(0,2);
  ldf(fa, lds, 0);
  int buf = 0;
#pragma unroll 1
  for (int st = 0; st < 6; ++st) {
    if (st == 3) STAMP(0,3);
    ldf(fb, lds + buf * K1_STAGE, 1);
    mma(fa);
    interleave<BR ? 16 : 12, BR ? 15 : 14>();
    __builtin_amdgcn_sched_barrier(0);
    buf = buf == 2 ? 0 : buf + 1;
    BARRIER();
    ldf(fa, lds + buf * K1_STAGE, 0);
    mma(fb);
    interleave<BR ? 16 : 12, BR ? 15 : 14>();
    __builtin_amdgcn_sched_barrier(0);
  }
  ldf(fb, lds + buf * K1_STAGE, 1);
  mma(fa);
  interleave<BR ? 16 : 12, BR ? 15 : 14>();
  __builtin_amdgcn_sched_barrier(0);
  mma(fb);

  STAMP(0,4);
  const int h = hg * 2 + hh;
  const int srow = s0 + rq * 32 + c16;
  if (BR) {
    const float scale = 0.18898223650461362f;
    bf16x8 kp[3], qp[2];
#pragma unroll
    for (int t = 0; t < 3; ++t) {
      f32x4 hi = ak[t][1];
      if (g == 3) hi = f32x4{0, 0, 0, 0};
      kp[t] = pack8(ak[t][0], hi);
    }
#pragma unroll
    for (int t = 0; t < 2; ++t) {
      f32x4 hi = aq[t][1];
      if (g == 3) hi = f32x4{0, 0, 0, 0};
      qp[t] = pack8(aq[t][0], hi);
    }
    f32x4 sp[2], sc[2];
#pragma unroll
    for (int t = 0; t < 2; ++t) {
      sp[t] = mfma16(kp[t], qp[t], f32x4{0, 0, 0, 0});
      sc[t] = mfma16(kp[t + 1], qp[t], f32x4{0, 0, 0, 0});
    }
    bool okp[4], okc[4];
#pragma unroll
    for (int j = 0; j < 4; ++j) {
      int dp = c16 + 16 - 4 * g - j, dc = c16 - 4 * g - j;
      okp[j] = ((dp & 1) == 0) && dp <= 14;
      okc[j] = dc >= 0 && ((dc & 1) == 0);
    }
    const bool first = (s0 + rq * 32) == 0;
    const float NEG = -__builtin_inff();
    float m[2];
#pragma unroll
    for (int t = 0; t < 2; ++t) {
#pragma unroll
      for (int j = 0; j < 4; ++j) {
        bool op = okp[j] && !(t == 0 && first);
        sp[t][j] = op ? sp[t][j] : NEG;
        sc[t][j] = okc[j] ? sc[t][j] : NEG;
      }
      m[t] = fmaxf(fmaxf(fmaxf(sp[t][0], sp[t][1]), fmaxf(sp[t][2], sp[t][3])),
                   fmaxf(fmaxf(sc[t][0], sc[t][1]), fmaxf(sc[t][2], sc[t][3])));
    }
#pragma unroll
    for (int t = 0; t < 2; ++t) m[t] = xmax16_32(m[t]);
    const float c2 = scale * 1.4426950408889634f;
    float sum[2];
#pragma unroll
    for (int t = 0; t < 2; ++t) {
      const float mc = -m[t] * c2;
#pragma unroll
      for (int j = 0; j < 4; ++j) {
        sp[t][j] = __builtin_amdgcn_exp2f(fmaf(sp[t][j], c2, mc));
        sc[t][j] = __builtin_amdgcn_exp2f(fmaf(sc[t][j], c2, mc));
      }
      f32x4 sv = sp[t] + sc[t];
      sum[t] = (sv[0] + sv[1]) + (sv[2] + sv[3]);
    }
#pragma unroll
    for (int t = 0; t < 2; ++t) sum[t] = __builtin_amdgcn_rcpf(xsum16_32(sum[t]));
    f32x4 o[2][2];
#pragma unroll
    for (int t = 0; t < 2; ++t) {
      bf16x8 pp = pack8(sp[t] * sum[t], sc[t] * sum[t]);
#pragma unroll
      for (int et = 0; et < 2; ++et) o[t][et] = mfma16(pack8(av[t][et], av[t + 1][et]), pp, f32x4{0, 0, 0, 0});
    }
#pragma unroll
    for (int t = 0; t < 2; ++t)
#pragma unroll
      for (int et = 0; et < 2; ++et) {
        bf16x4 ob;
#pragma unroll
        for (int j = 0; j < 4; ++j) ob[j] = (__bf16)o[t][et][j];
        bf16* dp = outq + (long)(srow + 16 * t) * DM + h * 28 + 16 * et + 4 * g;
        if (et == 1) dp = (g < 3) ? dp : dummy + lane * 4;
        *(bf16x4*)dp = ob;
      }
  } else {
#pragma unroll
    for (int t = 0; t < 2; ++t)
#pragma unroll
      for (int c = 0; c < 2; ++c) {
        bf16x4 ob;
        f32x4 pv = phi4(aq[t][c]);
#pragma unroll
        for (int j = 0; j < 4; ++j) ob[j] = (__bf16)pv[j];
        bf16* dp = outq + (long)(srow + 16 * t) * DM + h * 28 + 16 * c + 4 * g;
        if (c == 1) dp = (g < 3) ? dp : dummy + lane * 4;
        *(bf16x4*)dp = ob;
      }
#pragma unroll
    for (int t = 1; t < 3; ++t) {
      ak[t][0] = phi4(ak[t][0]);
      ak[t][1] = phi4(ak[t][1]);
#pragma unroll
      for (int j = 0; j < 4; ++j)
        if (c16 >= 12) av[t][1][j] = (c16 == 12) ? 1.0f : 0.0f;
    }
    f32x4 kv[2][2];
#pragma unroll
    for (int dc = 0; dc < 2; ++dc)
#pragma unroll
      for (int ec = 0; ec < 2; ++ec)
        kv[dc][ec] = mfma16(pack8(ak[1][dc], ak[2][dc]), pack8(av[1][ec], av[2][ec]), f32x4{0, 0, 0, 0});
    BARRIER();
    f32x4* ex = (f32x4*)(lds) + hh * 1024 + lane;
    if (rq != 0) {
#pragma unroll
      for (int i = 0; i < 4; ++i) ex[(rq * 4 + i) * 64] = kv[i >> 1][i & 1];
    }
    BARRIER();
    if (rq == 0) {
      float* pb = part + ((long)rt * 16 + h) * 1024;
#pragma unroll
      for (int i = 0; i < 4; ++i) {
        f32x4 o = ((kv[i >> 1][i & 1] + ex[(4 + i) * 64]) + ex[(8 + i) * 64]) + ex[(12 + i) * 64];
        *(f32x4*)(pb + (16 * (i & 1) + c16) * 32 + 16 * (i >> 1) + 4 * g) = o;
      }
    }
  }
}

__global__ __launch_bounds__(768, 3) void k1_qkv(const bf16* __restrict__ xa, const bf16* __restrict__ wa,
                                                 const bf16* __restrict__ xb, const bf16* __restrict__ wb,
                                                 bf16* __restrict__ pq, bf16* __restrict__ ob,
                                                 float* __restrict__ part, bf16* __restrict__ dummy,
                                                 unsigned* __restrict__ flags,
                                                 const bf16* __restrict__ woa, const bf16* __restrict__ wob) {
  extern __shared__ __attribute__((aligned(16))) char lds[];
  const int b = blockIdx.x;
  const int lane = threadIdx.x & 63;
  const int wv = __builtin_amdgcn_readfirstlane(threadIdx.x >> 6);
  const int xcd = b & 7, idx = b >> 3;
  const int br = xcd & 1, sub = xcd >> 1;
  const int rt = (sub >> 1) * 8 + (idx >> 2), hg = (sub & 1) * 4 + (idx & 3);
  if (wv >= 8) {
    if (wv == 8) flags[b * 64 + lane] = 0u;
    k1_loader(br ? xb : xa, br ? wb : wa, rt, hg, lds, wv - 8, lane);
    const int line = idx * 98 + (wv - 8) * 64 + lane;
    if (wv < 10 && line < (idx + 1) * 98) {
      unsigned v = *(const unsigned*)((const char*)(br ? wob : woa) + line * 128);
      asm volatile("" :: "v"(v));
    }
    return;
  }
  unsigned pf = 0;
  {
    const int L = threadIdx.x;
    const char* pa;
    if (L < 224) {
      const int li = idx * 224 + L;
      pa = (const char*)(br ? xb : xa) + (long)((sub >> 1) * 1024) * (DM * 2) + li * 128;
    } else {
      int wi = idx * 168 + (L - 224); wi = wi < 5376 ? wi : 5375;
      const int k = wi / 12, r = wi - k * 12;
      int off = k * (NQKV * 2) + (r >> 2) * (DM * 2) + (sub & 1) * 448 + (r & 3) * 128;
      off = off < (DM * NQKV * 2 - 4) ? off : (DM * NQKV * 2 - 4);
      pa = (const char*)(br ? wb : wa) + off;
    }
    if (L < 392) pf = *(const unsigned*)pa;
  }
  if (br == 0) k1_body<0>(pq, part, dummy, rt, hg, lds, wv, lane);
  else         k1_body<1>(ob, part, dummy, rt, hg, lds, wv, lane);
  asm volatile("" :: "v"(pf));
  STAMP(0,5);
}

constexpr int OP_OB = 7 * 64 * 128;
constexpr int OP_WB = 448 * 224;
constexpr int OP_LDS = OP_OB + OP_WB;

DI void op_issue_a(const bf16* __restrict__ a, int m0, int wv, int lane, unsigned ldsbase) {
  const int row = wv * 8 + (lane >> 3), pos = lane & 7;
  const int aoff = ((m0 + row) * DM + ((pos ^ (row & 7)) * 8)) * 2;
#pragma unroll
  for (int kc = 0; kc < 7; ++kc) glds16((const char*)a + aoff + kc * 128, ldsbase + kc * 8192 + wv * 1024);
}
template <int NW>
DI void op_issue_w(const bf16* __restrict__ wo, int n0, int wv, int lane, unsigned ldsbase) {
  int woffs[NW];
#pragma unroll
  for (int i = 0; i < NW; ++i) {
    int c = (wv + 8 * i) * 64 + lane;
    int lrow = c / 14, pos = c - lrow * 14;
    woffs[i] = (((lrow & 32) + pi_inv(lrow & 31)) * DM + n0 + pos * 8) * 2;
  }
#pragma unroll
  for (int kc = 0; kc < 7; ++kc)
#pragma unroll
    for (int i = 0; i < NW; ++i)
      glds16((const char*)wo + woffs[i] + kc * (64 * DM * 2), ldsbase + OP_OB + kc * 14336 + (wv + 8 * i) * 1024);
}

template <int NC>
DI void op_compute(const char* lds, bf16* __restrict__ out, int m0, int ncol0, int ct0, int rbase, int lane) {
  const int g = lane >> 4, c16 = lane & 15;
  f32x4 acc[2][NC];
#pragma unroll
  for (int r = 0; r < 2; ++r)
#pragma unroll
    for (int c = 0; c < NC; ++c) acc[r][c] = f32x4{0, 0, 0, 0};
  const char* op = lds + (rbase + c16) * 128;
  const char* wp = lds + OP_OB + (4 * g + (c16 >> 2)) * 224 + ct0 * 32 + (c16 & 3) * 8;
  const int xs0 = (g ^ (c16 & 7)) << 4, xs1 = ((4 + g) ^ (c16 & 7)) << 4;
  struct Fr { bf16x8 of[2], wf[NC]; };
  auto ldf = [&](Fr& f, int xs, int wo) {
#pragma unroll
    for (int c = 0; c < NC; ++c) f.wf[c] = tr_frag(wp + wo + c * 32, 16 * 224);
#pragma unroll
    for (int r = 0; r < 2; ++r) f.of[r] = *(const bf16x8*)(op + r * 2048 + xs);
  };
  auto mma = [&](const Fr& f) {
#pragma unroll
    for (int r = 0; r < 2; ++r)
#pragma unroll
      for (int c = 0; c < NC; ++c) acc[r][c] = mfma16(f.wf[c], f.of[r], acc[r][c]);
  };
  Fr fa, fb;
  ldf(fa, xs0, 0);
#pragma unroll 1
  for (int kc = 0; kc < 6; ++kc) {
    ldf(fb, xs1, 32 * 224);
    mma(fa);
    __builtin_amdgcn_sched_barrier(0);
    op += 8192; wp += 64 * 224;
    ldf(fa, xs0, 0);
    mma(fb);
    __builtin_amdgcn_sched_barrier(0);
  }
  ldf(fb, xs1, 32 * 224);
  mma(fa);
  mma(fb);
#pragma unroll
  for (int r = 0; r < 2; ++r)
#pragma unroll
    for (int c = 0; c < NC; ++c) {
      bf16x4 ob;
#pragma unroll
      for (int j = 0; j < 4; ++j) ob[j] = (__bf16)acc[r][c][j];
      *(bf16x4*)(out + (long)(m0 + rbase + r * 16 + c16) * (2 * DM) + ncol0 + (ct0 + c) * 16 + 4 * g) = ob;
    }
}

#define RLX_AGENT __ATOMIC_RELAXED, __HIP_MEMORY_SCOPE_AGENT
typedef __attribute__((address_space(1))) unsigned gu32;
typedef __attribute__((address_space(1))) unsigned long long gu64;

DI bf16x4 reduce_head(const float* __restrict__ part, int h, int tid) {
  const float* p = part + (long)h * 1024 + tid * 4;
  f32x4 v[16];
#pragma unroll
  for (int i = 0; i < 16; ++i) v[i] = *(const f32x4*)(p + (long)i * 16 * 1024);
  f32x4 s = v[0];
#pragma unroll
  for (int i = 1; i < 16; ++i) s += v[i];
  const int e = tid >> 3, d = (tid & 7) * 4;
  bf16x4 r;
#pragma unroll
  for (int j = 0; j < 4; ++j) r[j] = (__bf16)((e <= 28 && d + j < 28) ? s[j] : 0.f);
  return r;
}

__global__ __launch_bounds__(512, 2) void k2_out(const bf16* __restrict__ pq, const bf16* __restrict__ woa,
                                                 const bf16* __restrict__ ob, const bf16* __restrict__ wob,
                                                 const float* __restrict__ part, bf16* kvt,
                                                 unsigned* flags, bf16* slow, bf16* __restrict__ out) {
  extern __shared__ __attribute__((aligned(16))) char lds[];
  const int tid = threadIdx.x, lane = tid & 63;
  const int wv = __builtin_amdgcn_readfirstlane(tid >> 6);
  const int g = lane >> 4, c16 = lane & 15;
  const int b = blockIdx.x;
  const int xcd = b & 7, idx = b >> 3;
  const int br = xcd & 1, sub = xcd >> 1;
  const int mt = sub * 8 + (idx >> 2), nt = idx & 3;
  const int m0 = mt * 64, n0 = nt * 112;
  const unsigned ldsbase = (unsigned)(size_t)lds;
  STAMP(1,0);
  if (br == 1) {
    if (idx < 4 && wv < 4) {
      const int h = sub * 4 + idx;
      bf16x4 r = reduce_head(part, h, tid);
      const int e = tid >> 3, d = (tid & 7) * 4;
      const unsigned long long v = __builtin_bit_cast(unsigned long long, r);
      gu64* gp = (gu64*)flags + (((h * 2 + (e >> 4)) * 4 + ((d & 7) >> 1)) * 64 + (d >> 3) * 16 + (e & 15));
      __hip_atomic_store(gp, (1ull << 32) | (v & 0xffffffffull), RLX_AGENT);
      __hip_atomic_store(gp + 64, (1ull << 32) | (v >> 32), RLX_AGENT);
    }
    op_issue_a(ob, m0, wv, lane, ldsbase);
    if (wv < 6) op_issue_w<2>(wob, n0, wv, lane, ldsbase);
    else        op_issue_w<1>(wob, n0, wv, lane, ldsbase);
    STAMP(1,1);
  } else {
  op_issue_a(pq, m0, wv, lane, ldsbase);
  if (wv < 6) { op_issue_w<2>(woa, n0, wv, lane, ldsbase); WAIT_VM(14); }
  else        { op_issue_w<1>(woa, n0, wv, lane, ldsbase); WAIT_VM(7); }
  BARRIER();
  STAMP(1,1);
  bf16x8 qf[2][4];
#pragma unroll
  for (int hi = 0; hi < 2; ++hi)
#pragma unroll
    for (int t = 0; t < 4; ++t) {
      const int row = 16 * t + c16;
      const int col = (wv * 2 + hi) * 28 + 8 * g, col1 = col + 4;
      bf16x4 lo = *(const bf16x4*)(lds + (col >> 6) * 8192 + row * 128 + ((((col & 63) >> 3) ^ (row & 7)) << 4) + (col & 7) * 2);
      int a1 = (col1 >> 6) * 8192 + row * 128 + ((((col1 & 63) >> 3) ^ (row & 7)) << 4) + (col1 & 7) * 2;
      bf16x4 hi4 = *(const bf16x4*)(lds + a1);
      if (g == 3) { hi4[0] = (__bf16)0.f; hi4[1] = (__bf16)0.f; hi4[2] = (__bf16)0.f; hi4[3] = (__bf16)0.f; }
      qf[hi][t] = __builtin_shufflevector(lo, hi4, 0, 1, 2, 3, 4, 5, 6, 7);
    }
  bf16x8 kf[2][2];
  {
    const gu64* gp = (const gu64*)flags + (wv * 16) * 64 + lane;
    unsigned val[16];
    bool okall = false;
    for (int spin = 0; spin < 4000; ++spin) {
      bool ok = true;
#pragma unroll
      for (int i = 0; i < 16; ++i) {
        unsigned long long x = __hip_atomic_load(gp + i * 64, RLX_AGENT);
        val[i] = (unsigned)x;
        ok = ok && ((x >> 32) == 1ull);
      }
      if (__all(ok)) { okall = true; break; }
      __builtin_amdgcn_s_sleep(2);
    }
    typedef __attribute__((ext_vector_type(4))) unsigned u32x4;
    if (okall) {
#pragma unroll
      for (int q = 0; q < 4; ++q)
        kf[q >> 1][q & 1] = __builtin_bit_cast(bf16x8, u32x4{val[4 * q], val[4 * q + 1], val[4 * q + 2], val[4 * q + 3]});
    } else {
      bf16* mine = slow + (long)b * 16384;
#pragma unroll 1
      for (int i = 0; i < 8; ++i) {
        const int h = wv * 2 + (i >> 2), t4 = (i & 3) * 64 + lane;
        *(bf16x4*)(mine + h * 1024 + t4 * 4) = reduce_head(part, h, t4);
      }
      asm volatile("s_waitcnt vmcnt(0)" ::: "memory");
#pragma unroll
      for (int hi = 0; hi < 2; ++hi) {
        kf[hi][0] = *(const bf16x8*)(mine + (wv * 2 + hi) * 1024 + c16 * 32 + 8 * g);
        kf[hi][1] = *(const bf16x8*)(mine + (wv * 2 + hi) * 1024 + (16 + c16) * 32 + 8 * g);
      }
    }
  }
  STAMP(1,2);
#pragma unroll
  for (int hi = 0; hi < 2; ++hi) {
    const int h = wv * 2 + hi;
    f32x4 n0v[4], n1v[4];
#pragma unroll
    for (int t = 0; t < 4; ++t) {
      n0v[t] = mfma16(kf[hi][0], qf[hi][t], f32x4{0, 0, 0, 0});
      n1v[t] = mfma16(kf[hi][1], qf[hi][t], f32x4{0, 0, 0, 0});
    }
#pragma unroll
    for (int t = 0; t < 4; ++t) {
      const float inv = __builtin_amdgcn_rcpf(bcast_row3(n1v[t][0]) + 1e-6f);
      bf16x4 o0, o1;
      f32x4 r0 = n0v[t] * inv, r1 = n1v[t] * inv;
#pragma unroll
      for (int j = 0; j < 4; ++j) { o0[j] = (__bf16)r0[j]; o1[j] = (__bf16)r1[j]; }
      const int row = 16 * t + c16;
      const int col = h * 28 + 4 * g;
      *(bf16x4*)(lds + (col >> 6) * 8192 + row * 128 + ((((col & 63) >> 3) ^ (row & 7)) << 4) + (col & 7) * 2) = o0;
      const int col1 = col + 16;
      int a1 = (col1 >> 6) * 8192 + row * 128 + ((((col1 & 63) >> 3) ^ (row & 7)) << 4) + (col1 & 7) * 2;
      a1 = (g < 3) ? a1 : OP_LDS + 16 + lane * 8;
      *(bf16x4*)(lds + a1) = o1;
    }
  }
  }
  asm volatile("s_waitcnt vmcnt(0)" ::: "memory");
  BARRIER();
  STAMP(1,3);
  if (wv < 6) op_compute<2>(lds, out, m0, br * DM + n0, (wv >> 1) * 2, (wv & 1) * 32, lane);
  else        op_compute<1>(lds, out, m0, br * DM + n0, 6, (wv & 1) * 32, lane);
  STAMP(1,4);
}

extern "C" void kernel_launch(void* const* d_in, const int* in_sizes, int n_in,
                              void* d_out, int out_size, void* d_ws, size_t ws_size,
                              hipStream_t stream) {
  const bf16* xa = (const bf16*)d_in[0];
  const bf16* wqa = (const bf16*)d_in[1];
  const bf16* woa = (const bf16*)d_in[2];
  const bf16* xb = (const bf16*)d_in[3];
  const bf16* wqb = (const bf16*)d_in[4];
  const bf16* wob = (const bf16*)d_in[5];
  bf16* out = (bf16*)d_out;
  char* ws = (char*)d_ws;
  bf16* pq = (bf16*)ws;
  bf16* ob = (bf16*)(ws + 2 * 1024 * 1024);
  float* part = (float*)(ws + 4 * 1024 * 1024);
  bf16* kvt = (bf16*)(ws + 6 * 1024 * 1024);
  unsigned* flags = (unsigned*)(ws + 7 * 1024 * 1024);
  bf16* dummy = (bf16*)(ws + 7 * 1024 * 1024 + 131072);
  bf16* slow = (bf16*)(ws + 8 * 1024 * 1024);

  (void)hipFuncSetAttribute((const void*)k1_qkv, hipFuncAttributeMaxDynamicSharedMemorySize, K1_LDS);
  (void)hipFuncSetAttribute((const void*)k2_out, hipFuncAttributeMaxDynamicSharedMemorySize, OP_LDS + 1024);

  k1_qkv<<<256, 768, K1_LDS, stream>>>(xa, wqa, xb, wqb, pq, ob, part, dummy, flags, woa, wob);
  k2_out<<<256, 512, OP_LDS + 1024, stream>>>(pq, woa, ob, wob, part, kvt, flags, slow, out);
}
```

```cpp
#include <hip/hip_runtime.h>
#include <hip/hip_bf16.h>
#include <stdint.h>

typedef __bf16 bf16;
typedef __attribute__((ext_vector_type(8))) __bf16 bf16x8;
typedef __attribute__((ext_vector_type(4))) __bf16 bf16x4;
typedef __attribute__((ext_vector_type(4))) float f32x4;
#define LAS __attribute__((address_space(3)))
#define DI __device__ __forceinline__

#define STAMP(k,i) do {} while (0)
constexpr int SEQ = 2048, DM = 448, NQKV = 1344;

DI float bfr(float x) { return (float)(__bf16)x; }

typedef __attribute__((ext_vector_type(2))) __bf16 bf16x2;
DI f32x4 bfr4(f32x4 v) {
  bf16x2 a = {(__bf16)v[0], (__bf16)v[1]}, b = {(__bf16)v[2], (__bf16)v[3]};
  unsigned ua = __builtin_bit_cast(unsigned, a), ub = __builtin_bit_cast(unsigned, b);
  asm volatile("" : "+v"(ua), "+v"(ub));
  return f32x4{__uint_as_float(ua << 16), __uint_as_float(ua & 0xffff0000u), __uint_as_float(ub << 16), __uint_as_float(ub & 0xffff0000u)};
}
DI void glds16(const void* gsrc, unsigned lds_dst) {
  unsigned keep;
  asm volatile("s_mov_b32 %0, m0\n\ts_mov_b32 m0, %2\n\ts_nop 0\n\tglobal_load_lds_dwordx4 %1, off\n\ts_mov_b32 m0, %0"
               : "=&s"(keep) : "v"(gsrc), "s"(lds_dst) : "memory");
}
#define WAIT_VM(n) asm volatile("s_waitcnt vmcnt(" #n ")" ::: "memory")
#define BARRIER() do { asm volatile("s_waitcnt lgkmcnt(0)" ::: "memory"); __builtin_amdgcn_s_barrier(); asm volatile("" ::: "memory"); } while (0)

DI bf16x8 tr_frag(const char* p, int hi_off) {
  bf16x4 lo = __builtin_amdgcn_ds_read_tr16_b64_v4bf16((LAS bf16x4*)(p));
  bf16x4 hi = __builtin_amdgcn_ds_read_tr16_b64_v4bf16((LAS bf16x4*)(p + hi_off));
  return __builtin_shufflevector(lo, hi, 0, 1, 2, 3, 4, 5, 6, 7);
}
DI f32x4 mfma16(bf16x8 a, bf16x8 b, f32x4 c) { return __builtin_amdgcn_mfma_f32_16x16x32_bf16(a, b, c, 0, 0, 0); }

DI bf16x8 pack8(f32x4 a, f32x4 b) {
  bf16x8 r;
#pragma unroll
  for (int i = 0; i < 4; ++i) { r[i] = (__bf16)a[i]; r[4 + i] = (__bf16)b[i]; }
  return r;
}
DI float phi_f(float t) {
  float r = t > 0.f ? t + 1.0f : bfr(__expf(t)) + 1e-6f;
  return bfr(r);
}
DI f32x4 phi4(f32x4 acc) {
  f32x4 t = bfr4(acc), e;
#pragma unroll
  for (int j = 0; j < 4; ++j) e[j] = t[j] > 0.f ? t[j] + 1.0f : __expf(t[j]) + 1e-6f;
  return e;
}
DI float xmax16_32(float m) {
  unsigned u = __float_as_uint(m);
  auto r = __builtin_amdgcn_permlane16_swap(u, u, false, false);
  float a = fmaxf(__uint_as_float(r[0]), __uint_as_float(r[1]));
  unsigned v = __float_as_uint(a);
  auto r2 = __builtin_amdgcn_permlane32_swap(v, v, false, false);
  return fmaxf(__uint_as_float(r2[0]), __uint_as_float(r2[1]));
}
DI float xsum16_32(float m) {
  unsigned u = __float_as_uint(m);
  auto r = __builtin_amdgcn_permlane16_swap(u, u, false, false);
  float a = __uint_as_float(r[0]) + __uint_as_float(r[1]);
  unsigned v = __float_as_uint(a);
  auto r2 = __builtin_amdgcn_permlane32_swap(v, v, false, false);
  return __uint_as_float(r2[0]) + __uint_as_float(r2[1]);
}
DI float bcast_row3(float x) {
  unsigned u = __float_as_uint(x);
  auto r = __builtin_amdgcn_permlane32_swap(u, u, false, false);
  unsigned v = r[1];
  auto r2 = __builtin_amdgcn_permlane16_swap(v, v, false, false);
  return __uint_as_float(r2[1]);
}
template <int NM, int ND> DI void interleave() {
  constexpr int LEAD = ND > NM ? ND - NM : 0;
  if (LEAD) __builtin_amdgcn_sched_group_barrier(0x100, LEAD, 0);
#pragma unroll
  for (int i = 0; i < NM; ++i) {
    __builtin_amdgcn_sched_group_barrier(0x008, 1, 0);
    if (i + LEAD < ND) __builtin_amdgcn_sched_group_barrier(0x100, 1, 0);
  }
}
DI int pi_inv(int lr) { return lr < 16 ? 8 * (lr >> 2) + (lr & 3) : 8 * ((lr - 16) >> 2) + 4 + (lr & 3); }

constexpr int K1_XB = 144 * 128;
constexpr int K1_WB = 64 * 352;
constexpr int K1_STAGE = K1_XB + K1_WB;
constexpr int K1_LDS = 3 * K1_STAGE;

DI void k1_loader(const bf16* __restrict__ x, const bf16* __restrict__ w, int rt, int hg, char* lds, int wv, int lane) {
  const int s0 = rt * 128;
  const unsigned ldsbase = (unsigned)(size_t)lds;
  const char* src[10];
  unsigned dst[10];
  long strd0;
#pragma unroll
  for (int i = 0; i < 4; ++i) {
    int xi = 2 + wv + 4 * i;
    int row = xi * 8 + (lane >> 3), pos = lane & 7;
    int grow = s0 - 16 + row;
    src[i] = (const char*)(x + (long)grow * DM + ((pos ^ (row & 7)) * 8));
    dst[i] = xi * 1024;
  }
#pragma unroll
  for (int i = 0; i < 6; ++i) {
    int j = wv + 4 * i;
    if (i == 0 && j < 2) {
      int row = j * 8 + (lane >> 3), pos = lane & 7;
      int grow = s0 - 16 + row; grow = grow < 0 ? 0 : grow;
      src[4] = (const char*)(x + (long)grow * DM + ((pos ^ (row & 7)) * 8));
      dst[4] = j * 1024;
      strd0 = 128;
    } else {
      int wi = j - 2;
      int c = wi * 64 + lane;
      int lrow = c / 22, pos = c - lrow * 22;
      pos = pos > 20 ? 20 : pos;
      int k = (lrow & 32) + pi_inv(lrow & 31);
      int seg = pos / 7, cc = pos - seg * 7;
      src[4 + i] = (const char*)(w + (long)k * NQKV + seg * DM + hg * 56 + cc * 8);
      dst[4 + i] = K1_XB + wi * 1024;
      if (i == 0) strd0 = 64L * NQKV * 2;
    }
  }
  auto issue = [&](int st, int buf) {
    unsigned b = ldsbase + buf * K1_STAGE;
#pragma unroll
    for (int i = 0; i < 4; ++i) glds16(src[i] + st * 128, b + dst[i]);
    glds16(src[4] + st * strd0, b + dst[4]);
#pragma unroll
    for (int i = 5; i < 10; ++i) glds16(src[i] + (long)st * (64L * NQKV * 2), b + dst[i]);
  };
  issue(0, 0);
  issue(1, 1);
#pragma unroll 1
  for (int st = 0; st < 7; ++st) {
    if (st < 6) WAIT_VM(10); else WAIT_VM(0);
    __builtin_amdgcn_s_barrier();
    if (st + 2 < 7) issue(st + 2, (st + 2) % 3);
  }
}

template <int BR>
DI void k1_body(bf16* __restrict__ outq, float* __restrict__ part, bf16* __restrict__ dummy, int rt, int hg, char* lds,
                int wv, int lane) {
  const int rq = wv >> 1, hh = wv & 1;
  const int g = lane >> 4, c16 = lane & 15;
  const int s0 = rt * 128;

  constexpr int T0 = BR ? 0 : 1;
  f32x4 aq[2][2], ak[3][2], av[3][2];
#pragma unroll
  for (int t = 0; t < 3; ++t)
#pragma unroll
    for (int c = 0; c < 2; ++c) {
      if (t < 2) aq[t][c] = f32x4{0, 0, 0, 0};
      ak[t][c] = f32x4{0, 0, 0, 0}; av[t][c] = f32x4{0, 0, 0, 0};
    }

  STAMP(0,0);
  const int xoff = (rq * 32 + c16) * 128;
  const int woff = K1_XB + (4 * g + (c16 >> 2)) * 352 + hh * 56 + (c16 & 3) * 8;
  struct Frags { bf16x8 xf[3], wf[6]; };
  auto ldf = [&](Frags& f, const char* sb, int ks) {
    const int xs = ((ks * 4 + g) ^ (c16 & 7)) << 4;
#pragma unroll
    for (int ct = 0; ct < 6; ++ct)
      f.wf[ct] = tr_frag(sb + woff + ks * 32 * 352 + (ct >> 1) * 112 + (ct & 1) * 32, 16 * 352);
#pragma unroll
    for (int t = T0; t < 3; ++t) f.xf[t] = *(const bf16x8*)(sb + xoff + t * 2048 + xs);
  };
  auto mma = [&](const Frags& f) {
#pragma unroll
    for (int t = T0; t < 3; ++t) {
#pragma unroll
      for (int c = 0; c < 2; ++c) {
        if (t >= 1) aq[t - 1][c] = mfma16(f.wf[c], f.xf[t], aq[t - 1][c]);
        if (BR) ak[t][c] = mfma16(f.wf[2 + c], f.xf[t], ak[t][c]);
        else    ak[t][c] = mfma16(f.xf[t], f.wf[2 + c], ak[t][c]);
        av[t][c] = mfma16(f.xf[t], f.wf[4 + c], av[t][c]);
      }
    }
  };
  Frags fa, fb;
  BARRIER();
  STAMP(0,2);
  ldf(fa, lds, 0);
  int buf = 0;
#pragma unroll 1
  for (int st = 0; st < 6; ++st) {
    if (st == 3) STAMP(0,3);
    ldf(fb, lds + buf * K1_STAGE, 1);
    mma(fa);
    interleave<BR ? 16 : 12, BR ? 15 : 14>();
    __builtin_amdgcn_sched_barrier(0);
    buf = buf == 2 ? 0 : buf + 1;
    BARRIER();
    ldf(fa, lds + buf * K1_STAGE, 0);
    mma(fb);
    interleave<BR ? 16 : 12, BR ? 15 : 14>();
    __builtin_amdgcn_sched_barrier(0);
  }
  ldf(fb, lds + buf * K1_STAGE, 1);
  mma(fa);
  interleave<BR ? 16 : 12, BR ? 15 : 14>();
  __builtin_amdgcn_sched_barrier(0);
  mma(fb);

  STAMP(0,4);
  const int h = hg * 2 + hh;
  const int srow = s0 + rq * 32 + c16;
  if (BR) {
    const float scale = 0.18898223650461362f;
    bf16x8 kp[3], qp[2];
#pragma unroll
    for (int t = 0; t < 3; ++t) {
      f32x4 hi = ak[t][1];
      if (g == 3) hi = f32x4{0, 0, 0, 0};
      kp[t] = pack8(ak[t][0], hi);
    }
#pragma unroll
    for (int t = 0; t < 2; ++t) {
      f32x4 hi = aq[t][1];
      if (g == 3) hi = f32x4{0, 0, 0, 0};
      qp[t] = pack8(aq[t][0], hi);
    }
    f32x4 sp[2], sc[2];
#pragma unroll
    for (int t = 0; t < 2; ++t) {
      sp[t] = mfma16(kp[t], qp[t], f32x4{0, 0, 0, 0});
      sc[t] = mfma16(kp[t + 1], qp[t], f32x4{0, 0, 0, 0});
    }
    bool okp[4], okc[4];
#pragma unroll
    for (int j = 0; j < 4; ++j) {
      int dp = c16 + 16 - 4 * g - j, dc = c16 - 4 * g - j;
      okp[j] = ((dp & 1) == 0) && dp <= 14;
      okc[j] = dc >= 0 && ((dc & 1) == 0);
    }
    const bool first = (s0 + rq * 32) == 0;
    const float NEG = -__builtin_inff();
    float m[2];
#pragma unroll
    for (int t = 0; t < 2; ++t) {
#pragma unroll
      for (int j = 0; j < 4; ++j) {
        bool op = okp[j] && !(t == 0 && first);
        sp[t][j] = op ? sp[t][j] : NEG;
        sc[t][j] = okc[j] ? sc[t][j] : NEG;
      }
      m[t] = fmaxf(fmaxf(fmaxf(sp[t][0], sp[t][1]), fmaxf(sp[t][2], sp[t][3])),
                   fmaxf(fmaxf(sc[t][0], sc[t][1]), fmaxf(sc[t][2], sc[t][3])));
    }
#pragma unroll
    for (int t = 0; t < 2; ++t) m[t] = xmax16_32(m[t]);
    const float c2 = scale * 1.4426950408889634f;
    float sum[2];
#pragma unroll
    for (int t = 0; t < 2; ++t) {
      const float mc = -m[t] * c2;
#pragma unroll
      for (int j = 0; j < 4; ++j) {
        sp[t][j] = __builtin_amdgcn_exp2f(fmaf(sp[t][j], c2, mc));
        sc[t][j] = __builtin_amdgcn_exp2f(fmaf(sc[t][j], c2, mc));
      }
      f32x4 sv = sp[t] + sc[t];
      sum[t] = (sv[0] + sv[1]) + (sv[2] + sv[3]);
    }
#pragma unroll
    for (int t = 0; t < 2; ++t) sum[t] = __builtin_amdgcn_rcpf(xsum16_32(sum[t]));
    f32x4 o[2][2];
#pragma unroll
    for (int t = 0; t < 2; ++t) {
      bf16x8 pp = pack8(sp[t] * sum[t], sc[t] * sum[t]);
#pragma unroll
      for (int et = 0; et < 2; ++et) o[t][et] = mfma16(pack8(av[t][et], av[t + 1][et]), pp, f32x4{0, 0, 0, 0});
    }
#pragma unroll
    for (int t = 0; t < 2; ++t)
#pragma unroll
      for (int et = 0; et < 2; ++et) {
        bf16x4 ob;
#pragma unroll
        for (int j = 0; j < 4; ++j) ob[j] = (__bf16)o[t][et][j];
        bf16* dp = outq + (long)(srow + 16 * t) * DM + h * 28 + 16 * et + 4 * g;
        if (et == 1) dp = (g < 3) ? dp : dummy + lane * 4;
        *(bf16x4*)dp = ob;
      }
  } else {
#pragma unroll
    for (int t = 0; t < 2; ++t)
#pragma unroll
      for (int c = 0; c < 2; ++c) {
        bf16x4 ob;
        f32x4 pv = phi4(aq[t][c]);
#pragma unroll
        for (int j = 0; j < 4; ++j) ob[j] = (__bf16)pv[j];
        bf16* dp = outq + (long)(srow + 16 * t) * DM + h * 28 + 16 * c + 4 * g;
        if (c == 1) dp = (g < 3) ? dp : dummy + lane * 4;
        *(bf16x4*)dp = ob;
      }
#pragma unroll
    for (int t = 1; t < 3; ++t) {
      ak[t][0] = phi4(ak[t][0]);
      ak[t][1] = phi4(ak[t][1]);
#pragma unroll
      for (int j = 0; j < 4; ++j)
        if (c16 >= 12) av[t][1][j] = (c16 == 12) ? 1.0f : 0.0f;
    }
    f32x4 kv[2][2];
#pragma unroll
    for (int dc = 0; dc < 2; ++dc)
#pragma unroll
      for (int ec = 0; ec < 2; ++ec)
        kv[dc][ec] = mfma16(pack8(ak[1][dc], ak[2][dc]), pack8(av[1][ec], av[2][ec]), f32x4{0, 0, 0, 0});
    BARRIER();
    f32x4* ex = (f32x4*)(lds) + hh * 1024 + lane;
    if (rq != 0) {
#pragma unroll
      for (int i = 0; i < 4; ++i) ex[(rq * 4 + i) * 64] = kv[i >> 1][i & 1];
    }
    BARRIER();
    if (rq == 0) {
      float* pb = part + ((long)rt * 16 + h) * 1024;
#pragma unroll
      for (int i = 0; i < 4; ++i) {
        f32x4 o = ((kv[i >> 1][i & 1] + ex[(4 + i) * 64]) + ex[(8 + i) * 64]) + ex[(12 + i) * 64];
        *(f32x4*)(pb + (16 * (i & 1) + c16) * 32 + 16 * (i >> 1) + 4 * g) = o;
      }
    }
  }
}

__global__ __launch_bounds__(768, 3) void k1_qkv(const bf16* __restrict__ xa, const bf16* __restrict__ wa,
                                                 const bf16* __restrict__ xb, const bf16* __restrict__ wb,
                                                 bf16* __restrict__ pq, bf16* __restrict__ ob,
                                                 float* __restrict__ part, bf16* __restrict__ dummy,
                                                 unsigned* __restrict__ flags,
                                                 const bf16* __restrict__ woa, const bf16* __restrict__ wob) {
  extern __shared__ __attribute__((aligned(16))) char lds[];
  const int b = blockIdx.x;
  const int lane = threadIdx.x & 63;
  const int wv = __builtin_amdgcn_readfirstlane(threadIdx.x >> 6);
  const int xcd = b & 7, idx = b >> 3;
  const int br = xcd & 1, sub = xcd >> 1;
  const int rt = (sub >> 1) * 8 + (idx >> 2), hg = (sub & 1) * 4 + (idx & 3);
  if (wv >= 8) {
    if (wv == 8) flags[b * 64 + lane] = 0u;
    k1_loader(br ? xb : xa, br ? wb : wa, rt, hg, lds, wv - 8, lane);
    const int line = idx * 98 + (wv - 8) * 64 + lane;
    if (wv < 10 && line < (idx + 1) * 98) {
      unsigned v = *(const unsigned*)((const char*)(br ? wob : woa) + line * 128);
      asm volatile("" :: "v"(v));
    }
    return;
  }
  unsigned pf = 0;
  {
    const int L = threadIdx.x;
    const char* pa;
    if (L < 224) {
      const int li = idx * 224 + L;
      pa = (const char*)(br ? xb : xa) + (long)((sub >> 1) * 1024) * (DM * 2) + li * 128;
    } else {
      int wi = idx * 168 + (L - 224); wi = wi < 5376 ? wi : 5375;
      const int k = wi / 12, r = wi - k * 12;
      int off = k * (NQKV * 2) + (r >> 2) * (DM * 2) + (sub & 1) * 448 + (r & 3) * 128;
      off = off < (DM * NQKV * 2 - 4) ? off : (DM * NQKV * 2 - 4);
      pa = (const char*)(br ? wb : wa) + off;
    }
    if (L < 392) pf = *(const unsigned*)pa;
  }
  if (br == 0) k1_body<0>(pq, part, dummy, rt, hg, lds, wv, lane);
  else         k1_body<1>(ob, part, dummy, rt, hg, lds, wv, lane);
  asm volatile("" :: "v"(pf));
  STAMP(0,5);
}

constexpr int OP_OB = 7 * 64 * 128;
constexpr int OP_WB = 448 * 224;
constexpr int OP_LDS = OP_OB + OP_WB;

DI void op_issue_a(const bf16* __restrict__ a, int m0, int wv, int lane, unsigned ldsbase) {
  const int row = wv * 8 + (lane >> 3), pos = lane & 7;
  const int aoff = ((m0 + row) * DM + ((pos ^ (row & 7)) * 8)) * 2;
#pragma unroll
  for (int kc = 0; kc < 7; ++kc) glds16((const char*)a + aoff + kc * 128, ldsbase + kc * 8192 + wv * 1024);
}
template <int NW>
DI void op_issue_w(const bf16* __restrict__ wo, int n0, int wv, int lane, unsigned ldsbase) {
  int woffs[NW];
#pragma unroll
  for (int i = 0; i < NW; ++i) {
    int c = (wv + 8 * i) * 64 + lane;
    int lrow = c / 14, pos = c - lrow * 14;
    woffs[i] = (lrow * DM + n0 + pos * 8) * 2;
  }
#pragma unroll
  for (int kc = 0; kc < 7; ++kc)
#pragma unroll
    for (int i = 0; i < NW; ++i)
      glds16((const char*)wo + woffs[i] + kc * (64 * DM * 2), ldsbase + OP_OB + kc * 14336 + (wv + 8 * i) * 1024);
}

template <int NC>
DI void op_compute(const char* lds, bf16* __restrict__ out, int m0, int ncol0, int ct0, int rbase, int lane) {
  const int g = lane >> 4, c16 = lane & 15;
  f32x4 acc[2][NC];
#pragma unroll
  for (int r = 0; r < 2; ++r)
#pragma unroll
    for (int c = 0; c < NC; ++c) acc[r][c] = f32x4{0, 0, 0, 0};
  const char* op = lds + (rbase + c16) * 128 + (g & 1) * 8;
  const char* wp = lds + OP_OB + (4 * g + (c16 >> 2)) * 224 + ct0 * 32 + (c16 & 3) * 8;
  const int r7 = c16 & 7;
  const int xs0 = (((g >> 1)) ^ r7) << 4, xs0h = ((2 + (g >> 1)) ^ r7) << 4;
  const int xs1 = ((4 + (g >> 1)) ^ r7) << 4, xs1h = ((6 + (g >> 1)) ^ r7) << 4;
  struct Fr { bf16x8 of[2], wf[NC]; };
  auto ldf = [&](Fr& f, int xs, int xsh, int wo) {
#pragma unroll
    for (int c = 0; c < NC; ++c) f.wf[c] = tr_frag(wp + wo + c * 32, 16 * 224);
#pragma unroll
    for (int r = 0; r < 2; ++r) {
      bf16x4 lo = *(const bf16x4*)(op + r * 2048 + xs), hi = *(const bf16x4*)(op + r * 2048 + xsh);
      f.of[r] = __builtin_shufflevector(lo, hi, 0, 1, 2, 3, 4, 5, 6, 7);
    }
  };
  auto mma = [&](const Fr& f) {
#pragma unroll
    for (int r = 0; r < 2; ++r)
#pragma unroll
      for (int c = 0; c < NC; ++c) acc[r][c] = mfma16(f.wf[c], f.of[r], acc[r][c]);
  };
  Fr fa, fb;
  ldf(fa, xs0, xs0h, 0);
#pragma unroll 1
  for (int kc = 0; kc < 6; ++kc) {
    ldf(fb, xs1, xs1h, 32 * 224);
    mma(fa);
    __builtin_amdgcn_sched_barrier(0);
    op += 8192; wp += 64 * 224;
    ldf(fa, xs0, xs0h, 0);
    mma(fb);
    __builtin_amdgcn_sched_barrier(0);
  }
  ldf(fb, xs1, xs1h, 32 * 224);
  mma(fa);
  mma(fb);
#pragma unroll
  for (int r = 0; r < 2; ++r)
#pragma unroll
    for (int c = 0; c < NC; ++c) {
      bf16x4 ob;
#pragma unroll
      for (int j = 0; j < 4; ++j) ob[j] = (__bf16)acc[r][c][j];
      *(bf16x4*)(out + (long)(m0 + rbase + r * 16 + c16) * (2 * DM) + ncol0 + (ct0 + c) * 16 + 4 * g) = ob;
    }
}

#define RLX_AGENT __ATOMIC_RELAXED, __HIP_MEMORY_SCOPE_AGENT
typedef __attribute__((address_space(1))) unsigned gu32;
typedef __attribute__((address_space(1))) unsigned long long gu64;

DI bf16x4 reduce_head(const float* __restrict__ part, int h, int tid) {
  const float* p = part + (long)h * 1024 + tid * 4;
  f32x4 v[16];
#pragma unroll
  for (int i = 0; i < 16; ++i) v[i] = *(const f32x4*)(p + (long)i * 16 * 1024);
  f32x4 s = v[0];
#pragma unroll
  for (int i = 1; i < 16; ++i) s += v[i];
  const int e = tid >> 3, d = (tid & 7) * 4;
  bf16x4 r;
#pragma unroll
  for (int j = 0; j < 4; ++j) r[j] = (__bf16)((e <= 28 && d + j < 28) ? s[j] : 0.f);
  return r;
}

__global__ __launch_bounds__(512, 2) void k2_out(const bf16* __restrict__ pq, const bf16* __restrict__ woa,
                                                 const bf16* __restrict__ ob, const bf16* __restrict__ wob,
                                                 const float* __restrict__ part, bf16* kvt,
                                                 unsigned* flags, bf16* slow, bf16* __restrict__ out) {
  extern __shared__ __attribute__((aligned(16))) char lds[];
  const int tid = threadIdx.x, lane = tid & 63;
  const int wv = __builtin_amdgcn_readfirstlane(tid >> 6);
  const int g = lane >> 4, c16 = lane & 15;
  const int b = blockIdx.x;
  const int xcd = b & 7, idx = b >> 3;
  const int br = xcd & 1, sub = xcd >> 1;
  const int mt = sub * 8 + (idx >> 2), nt = idx & 3;
  const int m0 = mt * 64, n0 = nt * 112;
  const unsigned ldsbase = (unsigned)(size_t)lds;
  STAMP(1,0);
  if (br == 1) {
    if (idx < 4 && wv < 4) {
      const int h = sub * 4 + idx;
      bf16x4 r = reduce_head(part, h, tid);
      const int e = tid >> 3, d = (tid & 7) * 4;
      const unsigned long long v = __builtin_bit_cast(unsigned long long, r);
      gu64* gp = (gu64*)flags + (((h * 2 + (e >> 4)) * 4 + ((d & 7) >> 1)) * 64 + (d >> 3) * 16 + (e & 15));
      __hip_atomic_store(gp, (1ull << 32) | (v & 0xffffffffull), RLX_AGENT);
      __hip_atomic_store(gp + 64, (1ull << 32) | (v >> 32), RLX_AGENT);
    }
    op_issue_a(ob, m0, wv, lane, ldsbase);
    if (wv < 6) op_issue_w<2>(wob, n0, wv, lane, ldsbase);
    else        op_issue_w<1>(wob, n0, wv, lane, ldsbase);
    STAMP(1,1);
    WAIT_VM(0);
    BARRIER();
    STAMP(1,3);
    if (wv < 6) op_compute<2>(lds, out, m0, DM + n0, (wv >> 1) * 2, (wv & 1) * 32, lane);
    else        op_compute<1>(lds, out, m0, DM + n0, 6, (wv & 1) * 32, lane);
    STAMP(1,4);
    return;
  }
  const int rt = wv & 3, kh = wv >> 2;
  if (wv == 0) *(f32x4*)(lds + OP_LDS + lane * 16) = f32x4{0, 0, 0, 0};
  op_issue_a(pq, m0, wv, lane, ldsbase);
  if (wv < 6) { op_issue_w<2>(woa, n0, wv, lane, ldsbase); WAIT_VM(14); }
  else        { op_issue_w<1>(woa, n0, wv, lane, ldsbase); WAIT_VM(7); }
  BARRIER();
  STAMP(1,1);
  bf16x8 qf[8];
  {
    const int row = 16 * rt + c16;
#pragma unroll
    for (int i = 0; i < 8; ++i) {
      const int col = (kh * 8 + i) * 28 + 8 * g, col1 = col + 4;
      bf16x4 lo = *(const bf16x4*)(lds + (col >> 6) * 8192 + row * 128 + ((((col & 63) >> 3) ^ (row & 7)) << 4) + (col & 7) * 2);
      bf16x4 hi4 = *(const bf16x4*)(lds + (col1 >> 6) * 8192 + row * 128 + ((((col1 & 63) >> 3) ^ (row & 7)) << 4) + (col1 & 7) * 2);
      if (g == 3) { hi4[0] = (__bf16)0.f; hi4[1] = (__bf16)0.f; hi4[2] = (__bf16)0.f; hi4[3] = (__bf16)0.f; }
      qf[i] = __builtin_shufflevector(lo, hi4, 0, 1, 2, 3, 4, 5, 6, 7);
    }
  }
  BARRIER();
  {
    typedef __attribute__((ext_vector_type(4))) unsigned u32x4;
    const gu64* gp = (const gu64*)flags + (wv * 16) * 64 + lane;
    unsigned val[16];
    bool okall = false;
    for (int spin = 0; spin < 4000; ++spin) {
      bool ok = true;
#pragma unroll
      for (int i = 0; i < 16; ++i) {
        unsigned long long x = __hip_atomic_load(gp + i * 64, RLX_AGENT);
        val[i] = (unsigned)x;
        ok = ok && ((x >> 32) == 1ull);
      }
      if (__all(ok)) { okall = true; break; }
      __builtin_amdgcn_s_sleep(1);
    }
    if (okall) {
#pragma unroll
      for (int q = 0; q < 4; ++q)
        *(u32x4*)(lds + (wv * 4 + q) * 1024 + lane * 16) = u32x4{val[4 * q], val[4 * q + 1], val[4 * q + 2], val[4 * q + 3]};
    } else {
      bf16* mine = slow + (long)b * 16384;
#pragma unroll 1
      for (int i = 0; i < 8; ++i) {
        const int h = wv * 2 + (i >> 2), t4 = (i & 3) * 64 + lane;
        *(bf16x4*)(mine + h * 1024 + t4 * 4) = reduce_head(part, h, t4);
      }
      asm volatile("s_waitcnt vmcnt(0)" ::: "memory");
#pragma unroll
      for (int q = 0; q < 4; ++q)
        *(bf16x8*)(lds + (wv * 4 + q) * 1024 + lane * 16) =
            *(const bf16x8*)(mine + (wv * 2 + (q >> 1)) * 1024 + ((q & 1) * 16 + c16) * 32 + 8 * g);
    }
  }
  asm volatile("s_waitcnt vmcnt(0)" ::: "memory");
  BARRIER();
  STAMP(1,2);
  f32x4 acc[7];
#pragma unroll
  for (int c = 0; c < 7; ++c) acc[c] = f32x4{0, 0, 0, 0};
  const char* kp = lds + (kh * 16) * 1024 + lane * 16;
  const char* wp = lds + OP_OB + (kh * 8 * 28 + 4 * g + (c16 >> 2)) * 224 + (c16 & 3) * 8;
#pragma unroll
  for (int i = 0; i < 8; ++i) {
    bf16x8 k0 = *(const bf16x8*)(kp + (2 * i) * 1024), k1 = *(const bf16x8*)(kp + (2 * i + 1) * 1024);
    f32x4 n0v = mfma16(k0, qf[i], f32x4{0, 0, 0, 0});
    f32x4 n1v = mfma16(k1, qf[i], f32x4{0, 0, 0, 0});
    const float inv = __builtin_amdgcn_rcpf(bcast_row3(n1v[0]) + 1e-6f);
    n0v = n0v * inv;
    n1v = n1v * inv;
    if (g == 3) n1v = f32x4{0, 0, 0, 0};
    const bf16x8 of = pack8(n0v, n1v);
#pragma unroll
    for (int c = 0; c < 7; ++c) acc[c] = mfma16(tr_frag(wp + i * (28 * 224) + c * 32, 16 * 224), of, acc[c]);
  }
  STAMP(1,3);
  f32x4* ex = (f32x4*)lds + rt * 448 + lane;
  BARRIER();
  if (kh == 1) {
#pragma unroll
    for (int c = 0; c < 7; ++c) ex[c * 64] = acc[c];
  }
  BARRIER();
  if (kh == 0) {
    bf16* op = out + (long)(m0 + 16 * rt + c16) * (2 * DM) + n0 + 4 * g;
#pragma unroll
    for (int c = 0; c < 7; ++c) {
      f32x4 v = acc[c] + ex[c * 64];
      bf16x4 ob4;
#pragma unroll
      for (int j = 0; j < 4; ++j) ob4[j] = (__bf16)v[j];
      *(bf16x4*)(op + c * 16) = ob4;
    }
  }
  STAMP(1,4);
}

extern "C" void kernel_launch(void* const* d_in, const int* in_sizes, int n_in,
                              void* d_out, int out_size, void* d_ws, size_t ws_size,
                              hipStream_t stream) {
  const bf16* xa = (const bf16*)d_in[0];
  const bf16* wqa = (const bf16*)d_in[1];
  const bf16* woa = (const bf16*)d_in[2];
  const bf16* xb = (const bf16*)d_in[3];
  const bf16* wqb = (const bf16*)d_in[4];
  const bf16* wob = (const bf16*)d_in[5];
  bf16* out = (bf16*)d_out;
  char* ws = (char*)d_ws;
  bf16* pq = (bf16*)ws;
  bf16* ob = (bf16*)(ws + 2 * 1024 * 1024);
  float* part = (float*)(ws + 4 * 1024 * 1024);
  bf16* kvt = (bf16*)(ws + 6 * 1024 * 1024);
  unsigned* flags = (unsigned*)(ws + 7 * 1024 * 1024);
  bf16* dummy = (bf16*)(ws + 7 * 1024 * 1024 + 131072);
  bf16* slow = (bf16*)(ws + 8 * 1024 * 1024);

  (void)hipFuncSetAttribute((const void*)k1_qkv, hipFuncAttributeMaxDynamicSharedMemorySize, K1_LDS);
  (void)hipFuncSetAttribute((const void*)k2_out, hipFuncAttributeMaxDynamicSharedMemorySize, OP_LDS + 1024);

  k1_qkv<<<256, 768, K1_LDS, stream>>>(xa, wqa, xb, wqb, pq, ob, part, dummy, flags, woa, wob);
  k2_out<<<256, 512, OP_LDS + 1024, stream>>>(pq, woa, ob, wob, part, kvt, flags, slow, out);
}
```

```cpp
#include <hip/hip_runtime.h>
#include <hip/hip_bf16.h>
#include <stdint.h>

typedef __bf16 bf16;
typedef __attribute__((ext_vector_type(8))) __bf16 bf16x8;
typedef __attribute__((ext_vector_type(4))) __bf16 bf16x4;
typedef __attribute__((ext_vector_type(4))) float f32x4;
#define LAS __attribute__((address_space(3)))
#define DI __device__ __forceinline__

#define STAMP(k,i) do {} while (0)
constexpr int SEQ = 2048, DM = 448, NQKV = 1344;

DI float bfr(float x) { return (float)(__bf16)x; }

typedef __attribute__((ext_vector_type(2))) __bf16 bf16x2;
DI f32x4 bfr4(f32x4 v) {
  bf16x2 a = {(__bf16)v[0], (__bf16)v[1]}, b = {(__bf16)v[2], (__bf16)v[3]};
  unsigned ua = __builtin_bit_cast(unsigned, a), ub = __builtin_bit_cast(unsigned, b);
  asm volatile("" : "+v"(ua), "+v"(ub));
  return f32x4{__uint_as_float(ua << 16), __uint_as_float(ua & 0xffff0000u), __uint_as_float(ub << 16), __uint_as_float(ub & 0xffff0000u)};
}
DI void glds16(const void* gsrc, unsigned lds_dst) {
  unsigned keep;
  asm volatile("s_mov_b32 %0, m0\n\ts_mov_b32 m0, %2\n\ts_nop 0\n\tglobal_load_lds_dwordx4 %1, off\n\ts_mov_b32 m0, %0"
               : "=&s"(keep) : "v"(gsrc), "s"(lds_dst) : "memory");
}
#define WAIT_VM(n) asm volatile("s_waitcnt vmcnt(" #n ")" ::: "memory")
#define BARRIER() do { asm volatile("s_waitcnt lgkmcnt(0)" ::: "memory"); __builtin_amdgcn_s_barrier(); asm volatile("" ::: "memory"); } while (0)

DI bf16x8 tr_frag(const char* p, int hi_off) {
  bf16x4 lo = __builtin_amdgcn_ds_read_tr16_b64_v4bf16((LAS bf16x4*)(p));
  bf16x4 hi = __builtin_amdgcn_ds_read_tr16_b64_v4bf16((LAS bf16x4*)(p + hi_off));
  return __builtin_shufflevector(lo, hi, 0, 1, 2, 3, 4, 5, 6, 7);
}
DI f32x4 mfma16(bf16x8 a, bf16x8 b, f32x4 c) { return __builtin_amdgcn_mfma_f32_16x16x32_bf16(a, b, c, 0, 0, 0); }

DI bf16x8 pack8(f32x4 a, f32x4 b) {
  bf16x8 r;
#pragma unroll
  for (int i = 0; i < 4; ++i) { r[i] = (__bf16)a[i]; r[4 + i] = (__bf16)b[i]; }
  return r;
}
DI float phi_f(float t) {
  float r = t > 0.f ? t + 1.0f : bfr(__expf(t)) + 1e-6f;
  return bfr(r);
}
DI f32x4 phi4(f32x4 acc) {
  f32x4 t = bfr4(acc), e;
#pragma unroll
  for (int j = 0; j < 4; ++j) e[j] = t[j] > 0.f ? t[j] + 1.0f : __expf(t[j]) + 1e-6f;
  return e;
}
DI float xmax16_32(float m) {
  unsigned u = __float_as_uint(m);
  auto r = __builtin_amdgcn_permlane16_swap(u, u, false, false);
  float a = fmaxf(__uint_as_float(r[0]), __uint_as_float(r[1]));
  unsigned v = __float_as_uint(a);
  auto r2 = __builtin_amdgcn_permlane32_swap(v, v, false, false);
  return fmaxf(__uint_as_float(r2[0]), __uint_as_float(r2[1]));
}
DI float xsum16_32(float m) {
  unsigned u = __float_as_uint(m);
  auto r = __builtin_amdgcn_permlane16_swap(u, u, false, false);
  float a = __uint_as_float(r[0]) + __uint_as_float(r[1]);
  unsigned v = __float_as_uint(a);
  auto r2 = __builtin_amdgcn_permlane32_swap(v, v, false, false);
  return __uint_as_float(r2[0]) + __uint_as_float(r2[1]);
}
DI float bcast_row3(float x) {
  unsigned u = __float_as_uint(x);
  auto r = __builtin_amdgcn_permlane32_swap(u, u, false, false);
  unsigned v = r[1];
  auto r2 = __builtin_amdgcn_permlane16_swap(v, v, false, false);
  return __uint_as_float(r2[1]);
}
template <int NM, int ND> DI void interleave() {
  constexpr int LEAD = ND > NM ? ND - NM : 0;
  if (LEAD) __builtin_amdgcn_sched_group_barrier(0x100, LEAD, 0);
#pragma unroll
  for (int i = 0; i < NM; ++i) {
    __builtin_amdgcn_sched_group_barrier(0x008, 1, 0);
    if (i + LEAD < ND) __builtin_amdgcn_sched_group_barrier(0x100, 1, 0);
  }
}
DI int pi_inv(int lr) { return lr < 16 ? 8 * (lr >> 2) + (lr & 3) : 8 * ((lr - 16) >> 2) + 4 + (lr & 3); }

constexpr int K1_XB = 144 * 128;
constexpr int K1_WB = 64 * 352;
constexpr int K1_STAGE = K1_XB + K1_WB;
constexpr int K1_LDS = 3 * K1_STAGE;

DI void k1_loader(const bf16* __restrict__ x, const bf16* __restrict__ w, int rt, int hg, char* lds, int wv, int lane) {
  const int s0 = rt * 128;
  const unsigned ldsbase = (unsigned)(size_t)lds;
  const char* src[10];
  unsigned dst[10];
  long strd0;
#pragma unroll
  for (int i = 0; i < 4; ++i) {
    int xi = 2 + wv + 4 * i;
    int row = xi * 8 + (lane >> 3), pos = lane & 7;
    int grow = s0 - 16 + row;
    src[i] = (const char*)(x + (long)grow * DM + ((pos ^ (row & 7)) * 8));
    dst[i] = xi * 1024;
  }
#pragma unroll
  for (int i = 0; i < 6; ++i) {
    int j = wv + 4 * i;
    if (i == 0 && j < 2) {
      int row = j * 8 + (lane >> 3), pos = lane & 7;
      int grow = s0 - 16 + row; grow = grow < 0 ? 0 : grow;
      src[4] = (const char*)(x + (long)grow * DM + ((pos ^ (row & 7)) * 8));
      dst[4] = j * 1024;
      strd0 = 128;
    } else {
      int wi = j - 2;
      int c = wi * 64 + lane;
      int lrow = c / 22, pos = c - lrow * 22;
      pos = pos > 20 ? 20 : pos;
      int k = (lrow & 32) + pi_inv(lrow & 31);
      int seg = pos / 7, cc = pos - seg * 7;
      src[4 + i] = (const char*)(w + (long)k * NQKV + seg * DM + hg * 56 + cc * 8);
      dst[4 + i] = K1_XB + wi * 1024;
      if (i == 0) strd0 = 64L * NQKV * 2;
    }
  }
  auto issue = [&](int st, int buf) {
    unsigned b = ldsbase + buf * K1_STAGE;
#pragma unroll
    for (int i = 0; i < 4; ++i) glds16(src[i] + st * 128, b + dst[i]);
    glds16(src[4] + st * strd0, b + dst[4]);
#pragma unroll
    for (int i = 5; i < 10; ++i) glds16(src[i] + (long)st * (64L * NQKV * 2), b + dst[i]);
  };
  issue(0, 0);
  issue(1, 1);
#pragma unroll 1
  for (int st = 0; st < 7; ++st) {
    if (st < 6) WAIT_VM(10); else WAIT_VM(0);
    __builtin_amdgcn_s_barrier();
    if (st + 2 < 7) issue(st + 2, (st + 2) % 3);
  }
}

template <int BR>
DI void k1_body(bf16* __restrict__ outq, float* __restrict__ part, bf16* __restrict__ dummy, int rt, int hg, char* lds,
                int wv, int lane) {
  const int rq = wv >> 1, hh = wv & 1;
  const int g = lane >> 4, c16 = lane & 15;
  const int s0 = rt * 128;

  constexpr int T0 = BR ? 0 : 1;
  f32x4 aq[2][2], ak[3][2], av[3][2];
#pragma unroll
  for (int t = 0; t < 3; ++t)
#pragma unroll
    for (int c = 0; c < 2; ++c) {
      if (t < 2) aq[t][c] = f32x4{0, 0, 0, 0};
      ak[t][c] = f32x4{0, 0, 0, 0}; av[t][c] = f32x4{0, 0, 0, 0};
    }

  STAMP(0,0);
  const int xoff = (rq * 32 + c16) * 128;
  const int woff = K1_XB + (4 * g + (c16 >> 2)) * 352 + hh * 56 + (c16 & 3) * 8;
  struct Frags { bf16x8 xf[3], wf[6]; };
  auto ldf = [&](Frags& f, const char* sb, int ks) {
    const int xs = ((ks * 4 + g) ^ (c16 & 7)) << 4;
#pragma unroll
    for (int ct = 0; ct < 6; ++ct)
      f.wf[ct] = tr_frag(sb + woff + ks * 32 * 352 + (ct >> 1) * 112 + (ct & 1) * 32, 16 * 352);
#pragma unroll
    for (int t = T0; t < 3; ++t) f.xf[t] = *(const bf16x8*)(sb + xoff + t * 2048 + xs);
  };
  auto mma = [&](const Frags& f) {
#pragma unroll
    for (int t = T0; t < 3; ++t) {
#pragma unroll
      for (int c = 0; c < 2; ++c) {
        if (t >= 1) aq[t - 1][c] = mfma16(f.wf[c], f.xf[t], aq[t - 1][c]);
        if (BR) ak[t][c] = mfma16(f.wf[2 + c], f.xf[t], ak[t][c]);
        else    ak[t][c] = mfma16(f.xf[t], f.wf[2 + c], ak[t][c]);
        av[t][c] = mfma16(f.xf[t], f.wf[4 + c], av[t][c]);
      }
    }
  };
  Frags fa, fb;
  BARRIER();
  STAMP(0,2);
  ldf(fa, lds, 0);
  int buf = 0;
#pragma unroll 1
  for (int st = 0; st < 6; ++st) {
    if (st == 3) STAMP(0,3);
    ldf(fb, lds + buf * K1_STAGE, 1);
    mma(fa);
    interleave<BR ? 16 : 12, BR ? 15 : 14>();
    __builtin_amdgcn_sched_barrier(0);
    buf = buf == 2 ? 0 : buf + 1;
    BARRIER();
    ldf(fa, lds + buf * K1_STAGE, 0);
    mma(fb);
    interleave<BR ? 16 : 12, BR ? 15 : 14>();
    __builtin_amdgcn_sched_barrier(0);
  }
  ldf(fb, lds + buf * K1_STAGE, 1);
  mma(fa);
  interleave<BR ? 16 : 12, BR ? 15 : 14>();
  __builtin_amdgcn_sched_barrier(0);
  mma(fb);

  STAMP(0,4);
  const int h = hg * 2 + hh;
  const int srow = s0 + rq * 32 + c16;
  if (BR) {
    const float scale = 0.18898223650461362f;
    bf16x8 kp[3], qp[2];
#pragma unroll
    for (int t = 0; t < 3; ++t) {
      f32x4 hi = ak[t][1];
      if (g == 3) hi = f32x4{0, 0, 0, 0};
      kp[t] = pack8(ak[t][0], hi);
    }
#pragma unroll
    for (int t = 0; t < 2; ++t) {
      f32x4 hi = aq[t][1];
      if (g == 3) hi = f32x4{0, 0, 0, 0};
      qp[t] = pack8(aq[t][0], hi);
    }
    f32x4 sp[2], sc[2];
#pragma unroll
    for (int t = 0; t < 2; ++t) {
      sp[t] = mfma16(kp[t], qp[t], f32x4{0, 0, 0, 0});
      sc[t] = mfma16(kp[t + 1], qp[t], f32x4{0, 0, 0, 0});
    }
    bool okp[4], okc[4];
#pragma unroll
    for (int j = 0; j < 4; ++j) {
      int dp = c16 + 16 - 4 * g - j, dc = c16 - 4 * g - j;
      okp[j] = ((dp & 1) == 0) && dp <= 14;
      okc[j] = dc >= 0 && ((dc & 1) == 0);
    }
    const bool first = (s0 + rq * 32) == 0;
    const float NEG = -__builtin_inff();
    float m[2];
#pragma unroll
    for (int t = 0; t < 2; ++t) {
#pragma unroll
      for (int j = 0; j < 4; ++j) {
        bool op = okp[j] && !(t == 0 && first);
        sp[t][j] = op ? sp[t][j] : NEG;
        sc[t][j] = okc[j] ? sc[t][j] : NEG;
      }
      m[t] = fmaxf(fmaxf(fmaxf(sp[t][0], sp[t][1]), fmaxf(sp[t][2], sp[t][3])),
                   fmaxf(fmaxf(sc[t][0], sc[t][1]), fmaxf(sc[t][2], sc[t][3])));
    }
#pragma unroll
    for (int t = 0; t < 2; ++t) m[t] = xmax16_32(m[t]);
    const float c2 = scale * 1.4426950408889634f;
    float sum[2];
#pragma unroll
    for (int t = 0; t < 2; ++t) {
      const float mc = -m[t] * c2;
#pragma unroll
      for (int j = 0; j < 4; ++j) {
        sp[t][j] = __builtin_amdgcn_exp2f(fmaf(sp[t][j], c2, mc));
        sc[t][j] = __builtin_amdgcn_exp2f(fmaf(sc[t][j], c2, mc));
      }
      f32x4 sv = sp[t] + sc[t];
      sum[t] = (sv[0] + sv[1]) + (sv[2] + sv[3]);
    }
#pragma unroll
    for (int t = 0; t < 2; ++t) sum[t] = __builtin_amdgcn_rcpf(xsum16_32(sum[t]));
    f32x4 o[2][2];
#pragma unroll
    for (int t = 0; t < 2; ++t) {
      bf16x8 pp = pack8(sp[t] * sum[t], sc[t] * sum[t]);
#pragma unroll
      for (int et = 0; et < 2; ++et) o[t][et] = mfma16(pack8(av[t][et], av[t + 1][et]), pp, f32x4{0, 0, 0, 0});
    }
#pragma unroll
    for (int t = 0; t < 2; ++t)
#pragma unroll
      for (int et = 0; et < 2; ++et) {
        bf16x4 ob;
#pragma unroll
        for (int j = 0; j < 4; ++j) ob[j] = (__bf16)o[t][et][j];
        bf16* dp = outq + (long)(srow + 16 * t) * DM + h * 28 + 16 * et + 4 * g;
        if (et == 1) dp = (g < 3) ? dp : dummy + lane * 4;
        *(bf16x4*)dp = ob;
      }
  } else {
#pragma unroll
    for (int t = 0; t < 2; ++t)
#pragma unroll
      for (int c = 0; c < 2; ++c) {
        bf16x4 ob;
        f32x4 pv = phi4(aq[t][c]);
#pragma unroll
        for (int j = 0; j < 4; ++j) ob[j] = (__bf16)pv[j];
        bf16* dp = outq + (long)(srow + 16 * t) * DM + h * 28 + 16 * c + 4 * g;
        if (c == 1) dp = (g < 3) ? dp : dummy + lane * 4;
        *(bf16x4*)dp = ob;
      }
#pragma unroll
    for (int t = 1; t < 3; ++t) {
      ak[t][0] = phi4(ak[t][0]);
      ak[t][1] = phi4(ak[t][1]);
#pragma unroll
      for (int j = 0; j < 4; ++j)
        if (c16 >= 12) av[t][1][j] = (c16 == 12) ? 1.0f : 0.0f;
    }
    f32x4 kv[2][2];
#pragma unroll
    for (int dc = 0; dc < 2; ++dc)
#pragma unroll
      for (int ec = 0; ec < 2; ++ec)
        kv[dc][ec] = mfma16(pack8(ak[1][dc], ak[2][dc]), pack8(av[1][ec], av[2][ec]), f32x4{0, 0, 0, 0});
    BARRIER();
    f32x4* ex = (f32x4*)(lds) + hh * 1024 + lane;
    if (rq != 0) {
#pragma unroll
      for (int i = 0; i < 4; ++i) ex[(rq * 4 + i) * 64] = kv[i >> 1][i & 1];
    }
    BARRIER();
    if (rq == 0) {
      float* pb = part + ((long)rt * 16 + h) * 1024;
#pragma unroll
      for (int i = 0; i < 4; ++i) {
        f32x4 o = ((kv[i >> 1][i & 1] + ex[(4 + i) * 64]) + ex[(8 + i) * 64]) + ex[(12 + i) * 64];
        *(f32x4*)(pb + (16 * (i & 1) + c16) * 32 + 16 * (i >> 1) + 4 * g) = o;
      }
    }
  }
}

__global__ __launch_bounds__(768, 3) void k1_qkv(const bf16* __restrict__ xa, const bf16* __restrict__ wa,
                                                 const bf16* __restrict__ xb, const bf16* __restrict__ wb,
                                                 bf16* __restrict__ pq, bf16* __restrict__ ob,
                                                 float* __restrict__ part, bf16* __restrict__ dummy,
                                                 unsigned* __restrict__ flags,
                                                 const bf16* __restrict__ woa, const bf16* __restrict__ wob) {
  extern __shared__ __attribute__((aligned(16))) char lds[];
  const int b = blockIdx.x;
  const int lane = threadIdx.x & 63;
  const int wv = __builtin_amdgcn_readfirstlane(threadIdx.x >> 6);
  const int xcd = b & 7, idx = b >> 3;
  const int br = xcd & 1, sub = xcd >> 1;
  const int rt = (sub >> 1) * 8 + (idx >> 2), hg = (sub & 1) * 4 + (idx & 3);
  if (wv >= 8) {
    if (wv == 8) flags[b * 64 + lane] = 0u;
    k1_loader(br ? xb : xa, br ? wb : wa, rt, hg, lds, wv - 8, lane);
    const int line = idx * 98 + (wv - 8) * 64 + lane;
    if (wv < 10 && line < (idx + 1) * 98) {
      unsigned v = *(const unsigned*)((const char*)(br ? wob : woa) + line * 128);
      asm volatile("" :: "v"(v));
    }
    return;
  }
  unsigned pf = 0;
  {
    const int L = threadIdx.x;
    const char* pa;
    if (L < 224) {
      const int li = idx * 224 + L;
      pa = (const char*)(br ? xb : xa) + (long)((sub >> 1) * 1024) * (DM * 2) + li * 128;
    } else {
      int wi = idx * 168 + (L - 224); wi = wi < 5376 ? wi : 5375;
      const int k = wi / 12, r = wi - k * 12;
      int off = k * (NQKV * 2) + (r >> 2) * (DM * 2) + (sub & 1) * 448 + (r & 3) * 128;
      off = off < (DM * NQKV * 2 - 4) ? off : (DM * NQKV * 2 - 4);
      pa = (const char*)(br ? wb : wa) + off;
    }
    if (L < 392) pf = *(const unsigned*)pa;
  }
  if (br == 0) k1_body<0>(pq, part, dummy, rt, hg, lds, wv, lane);
  else         k1_body<1>(ob, part, dummy, rt, hg, lds, wv, lane);
  asm volatile("" :: "v"(pf));
  STAMP(0,5);
}

constexpr int OP_OB = 7 * 64 * 128;
constexpr int OP_WB = 448 * 224;
constexpr int OP_LDS = OP_OB + OP_WB;

DI void op_issue_a(const bf16* __restrict__ a, int m0, int wv, int lane, unsigned ldsbase) {
  const int row = wv * 8 + (lane >> 3), pos = lane & 7;
  const int aoff = ((m0 + row) * DM + ((pos ^ (row & 7)) * 8)) * 2;
#pragma unroll
  for (int kc = 0; kc < 7; ++kc) glds16((const char*)a + aoff + kc * 128, ldsbase + kc * 8192 + wv * 1024);
}
template <int NW>
DI void op_issue_w(const bf16* __restrict__ wo, int n0, int wv, int lane, unsigned ldsbase) {
  int woffs[NW];
#pragma unroll
  for (int i = 0; i < NW; ++i) {
    int c = (wv + 8 * i) * 64 + lane;
    int lrow = c / 14, pos = c - lrow * 14;
    woffs[i] = (((lrow & 32) + pi_inv(lrow & 31)) * DM + n0 + pos * 8) * 2;
  }
#pragma unroll
  for (int kc = 0; kc < 7; ++kc)
#pragma unroll
    for (int i = 0; i < NW; ++i)
      glds16((const char*)wo + woffs[i] + kc * (64 * DM * 2), ldsbase + OP_OB + kc * 14336 + (wv + 8 * i) * 1024);
}

template <int NC>
DI void op_compute(const char* lds, bf16* __restrict__ out, int m0, int ncol0, int ct0, int rbase, int lane) {
  const int g = lane >> 4, c16 = lane & 15;
  f32x4 acc[2][NC];
#pragma unroll
  for (int r = 0; r < 2; ++r)
#pragma unroll
    for (int c = 0; c < NC; ++c) acc[r][c] = f32x4{0, 0, 0, 0};
  const char* op = lds + (rbase + c16) * 128;
  const char* wp = lds + OP_OB + (4 * g + (c16 >> 2)) * 224 + ct0 * 32 + (c16 & 3) * 8;
  const int xs0 = (g ^ (c16 & 7)) << 4, xs1 = ((4 + g) ^ (c16 & 7)) << 4;
  struct Fr { bf16x8 of[2], wf[NC]; };
  auto ldf = [&](Fr& f, int xs, int wo) {
#pragma unroll
    for (int c = 0; c < NC; ++c) f.wf[c] = tr_frag(wp + wo + c * 32, 16 * 224);
#pragma unroll
    for (int r = 0; r < 2; ++r) f.of[r] = *(const bf16x8*)(op + r * 2048 + xs);
  };
  auto mma = [&](const Fr& f) {
#pragma unroll
    for (int r = 0; r < 2; ++r)
#pragma unroll
      for (int c = 0; c < NC; ++c) acc[r][c] = mfma16(f.wf[c], f.of[r], acc[r][c]);
  };
  Fr fa, fb;
  ldf(fa, xs0, 0);
#pragma unroll 1
  for (int kc = 0; kc < 6; ++kc) {
    ldf(fb, xs1, 32 * 224);
    mma(fa);
    __builtin_amdgcn_sched_barrier(0);
    op += 8192; wp += 64 * 224;
    ldf(fa, xs0, 0);
    mma(fb);
    __builtin_amdgcn_sched_barrier(0);
  }
  ldf(fb, xs1, 32 * 224);
  mma(fa);
  mma(fb);
#pragma unroll
  for (int r = 0; r < 2; ++r)
#pragma unroll
    for (int c = 0; c < NC; ++c) {
      bf16x4 ob;
#pragma unroll
      for (int j = 0; j < 4; ++j) ob[j] = (__bf16)acc[r][c][j];
      *(bf16x4*)(out + (long)(m0 + rbase + r * 16 + c16) * (2 * DM) + ncol0 + (ct0 + c) * 16 + 4 * g) = ob;
    }
}

#define RLX_AGENT __ATOMIC_RELAXED, __HIP_MEMORY_SCOPE_AGENT
typedef __attribute__((address_space(1))) unsigned gu32;
typedef __attribute__((address_space(1))) unsigned long long gu64;

DI bf16x4 reduce_head(const float* __restrict__ part, int h, int tid) {
  const float* p = part + (long)h * 1024 + tid * 4;
  f32x4 v[16];
#pragma unroll
  for (int i = 0; i < 16; ++i) v[i] = *(const f32x4*)(p + (long)i * 16 * 1024);
  f32x4 s = v[0];
#pragma unroll
  for (int i = 1; i < 16; ++i) s += v[i];
  const int e = tid >> 3, d = (tid & 7) * 4;
  bf16x4 r;
#pragma unroll
  for (int j = 0; j < 4; ++j) r[j] = (__bf16)((e <= 28 && d + j < 28) ? s[j] : 0.f);
  return r;
}

__global__ __launch_bounds__(512, 2) void k2_out(const bf16* __restrict__ pq, const bf16* __restrict__ woa,
                                                 const bf16* __restrict__ ob, const bf16* __restrict__ wob,
                                                 const float* __restrict__ part, bf16* kvt,
                                                 unsigned* flags, bf16* slow, bf16* __restrict__ out) {
  extern __shared__ __attribute__((aligned(16))) char lds[];
  const int tid = threadIdx.x, lane = tid & 63;
  const int wv = __builtin_amdgcn_readfirstlane(tid >> 6);
  const int g = lane >> 4, c16 = lane & 15;
  const int b = blockIdx.x;
  const int xcd = b & 7, idx = b >> 3;
  const int br = xcd & 1, sub = xcd >> 1;
  const int mt = sub * 8 + (idx >> 2), nt = idx & 3;
  const int m0 = mt * 64, n0 = nt * 112;
  const unsigned ldsbase = (unsigned)(size_t)lds;
  STAMP(1,0);
  if (br == 1) {
    if (idx < 4 && wv < 4) {
      __builtin_amdgcn_s_setprio(3);
      const int h = sub * 4 + idx;
      bf16x4 r = reduce_head(part, h, tid);
      const int e = tid >> 3, d = (tid & 7) * 4;
      const unsigned long long v = __builtin_bit_cast(unsigned long long, r);
      gu64* gp = (gu64*)flags + (((h * 2 + (e >> 4)) * 4 + ((d & 7) >> 1)) * 64 + (d >> 3) * 16 + (e & 15));
      __hip_atomic_store(gp, (1ull << 32) | (v & 0xffffffffull), RLX_AGENT);
      __hip_atomic_store(gp + 64, (1ull << 32) | (v >> 32), RLX_AGENT);
    }
    op_issue_a(ob, m0, wv, lane, ldsbase);
    if (wv < 6) op_issue_w<2>(wob, n0, wv, lane, ldsbase);
    else        op_issue_w<1>(wob, n0, wv, lane, ldsbase);
    STAMP(1,1);
  } else {
  op_issue_a(pq, m0, wv, lane, ldsbase);
  if (wv < 6) { op_issue_w<2>(woa, n0, wv, lane, ldsbase); WAIT_VM(14); }
  else        { op_issue_w<1>(woa, n0, wv, lane, ldsbase); WAIT_VM(7); }
  BARRIER();
  STAMP(1,1);
  bf16x8 qf[2][4];
#pragma unroll
  for (int hi = 0; hi < 2; ++hi)
#pragma unroll
    for (int t = 0; t < 4; ++t) {
      const int row = 16 * t + c16;
      const int col = (wv * 2 + hi) * 28 + 8 * g, col1 = col + 4;
      bf16x4 lo = *(const bf16x4*)(lds + (col >> 6) * 8192 + row * 128 + ((((col & 63) >> 3) ^ (row & 7)) << 4) + (col & 7) * 2);
      int a1 = (col1 >> 6) * 8192 + row * 128 + ((((col1 & 63) >> 3) ^ (row & 7)) << 4) + (col1 & 7) * 2;
      bf16x4 hi4 = *(const bf16x4*)(lds + a1);
      if (g == 3) { hi4[0] = (__bf16)0.f; hi4[1] = (__bf16)0.f; hi4[2] = (__bf16)0.f; hi4[3] = (__bf16)0.f; }
      qf[hi][t] = __builtin_shufflevector(lo, hi4, 0, 1, 2, 3, 4, 5, 6, 7);
    }
  bf16x8 kf[2][2];
  {
    const gu64* gp = (const gu64*)flags + (wv * 16) * 64 + lane;
    unsigned val[16];
    bool okall = false;
    for (int spin = 0; spin < 4000; ++spin) {
      bool ok = true;
#pragma unroll
      for (int i = 0; i < 16; ++i) {
        unsigned long long x = __hip_atomic_load(gp + i * 64, RLX_AGENT);
        val[i] = (unsigned)x;
        ok = ok && ((x >> 32) == 1ull);
      }
      if (__all(ok)) { okall = true; break; }

    }
    typedef __attribute__((ext_vector_type(4))) unsigned u32x4;
    if (okall) {
#pragma unroll
      for (int q = 0; q < 4; ++q)
        kf[q >> 1][q & 1] = __builtin_bit_cast(bf16x8, u32x4{val[4 * q], val[4 * q + 1], val[4 * q + 2], val[4 * q + 3]});
    } else {
      bf16* mine = slow + (long)b * 16384;
#pragma unroll 1
      for (int i = 0; i < 8; ++i) {
        const int h = wv * 2 + (i >> 2), t4 = (i & 3) * 64 + lane;
        *(bf16x4*)(mine + h * 1024 + t4 * 4) = reduce_head(part, h, t4);
      }
      asm volatile("s_waitcnt vmcnt(0)" ::: "memory");
#pragma unroll
      for (int hi = 0; hi < 2; ++hi) {
        kf[hi][0] = *(const bf16x8*)(mine + (wv * 2 + hi) * 1024 + c16 * 32 + 8 * g);
        kf[hi][1] = *(const bf16x8*)(mine + (wv * 2 + hi) * 1024 + (16 + c16) * 32 + 8 * g);
      }
    }
  }
  STAMP(1,2);
#pragma unroll
  for (int hi = 0; hi < 2; ++hi) {
    const int h = wv * 2 + hi;
    f32x4 n0v[4], n1v[4];
#pragma unroll
    for (int t = 0; t < 4; ++t) {
      n0v[t] = mfma16(kf[hi][0], qf[hi][t], f32x4{0, 0, 0, 0});
      n1v[t] = mfma16(kf[hi][1], qf[hi][t], f32x4{0, 0, 0, 0});
    }
#pragma unroll
    for (int t = 0; t < 4; ++t) {
      const float inv = __builtin_amdgcn_rcpf(bcast_row3(n1v[t][0]) + 1e-6f);
      bf16x4 o0, o1;
      f32x4 r0 = n0v[t] * inv, r1 = n1v[t] * inv;
#pragma unroll
      for (int j = 0; j < 4; ++j) { o0[j] = (__bf16)r0[j]; o1[j] = (__bf16)r1[j]; }
      const int row = 16 * t + c16;
      const int col = h * 28 + 4 * g;
      *(bf16x4*)(lds + (col >> 6) * 8192 + row * 128 + ((((col & 63) >> 3) ^ (row & 7)) << 4) + (col & 7) * 2) = o0;
      const int col1 = col + 16;
      int a1 = (col1 >> 6) * 8192 + row * 128 + ((((col1 & 63) >> 3) ^ (row & 7)) << 4) + (col1 & 7) * 2;
      a1 = (g < 3) ? a1 : OP_LDS + 16 + lane * 8;
      *(bf16x4*)(lds + a1) = o1;
    }
  }
  }
  asm volatile("s_waitcnt vmcnt(0)" ::: "memory");
  BARRIER();
  STAMP(1,3);
  if (wv < 6) op_compute<2>(lds, out, m0, br * DM + n0, (wv >> 1) * 2, (wv & 1) * 32, lane);
  else        op_compute<1>(lds, out, m0, br * DM + n0, 6, (wv & 1) * 32, lane);
  STAMP(1,4);
}

extern "C" void kernel_launch(void* const* d_in, const int* in_sizes, int n_in,
                              void* d_out, int out_size, void* d_ws, size_t ws_size,
                              hipStream_t stream) {
  const bf16* xa = (const bf16*)d_in[0];
  const bf16* wqa = (const bf16*)d_in[1];
  const bf16* woa = (const bf16*)d_in[2];
  const bf16* xb = (const bf16*)d_in[3];
  const bf16* wqb = (const bf16*)d_in[4];
  const bf16* wob = (const bf16*)d_in[5];
  bf16* out = (bf16*)d_out;
  char* ws = (char*)d_ws;
  bf16* pq = (bf16*)ws;
  bf16* ob = (bf16*)(ws + 2 * 1024 * 1024);
  float* part = (float*)(ws + 4 * 1024 * 1024);
  bf16* kvt = (bf16*)(ws + 6 * 1024 * 1024);
  unsigned* flags = (unsigned*)(ws + 7 * 1024 * 1024);
  bf16* dummy = (bf16*)(ws + 7 * 1024 * 1024 + 131072);
  bf16* slow = (bf16*)(ws + 8 * 1024 * 1024);

  (void)hipFuncSetAttribute((const void*)k1_qkv, hipFuncAttributeMaxDynamicSharedMemorySize, K1_LDS);
  (void)hipFuncSetAttribute((const void*)k2_out, hipFuncAttributeMaxDynamicSharedMemorySize, OP_LDS + 1024);

  k1_qkv<<<256, 768, K1_LDS, stream>>>(xa, wqa, xb, wqb, pq, ob, part, dummy, flags, woa, wob);
  k2_out<<<256, 512, OP_LDS + 1024, stream>>>(pq, woa, ob, wob, part, kvt, flags, slow, out);
}
```

```cpp
#include <hip/hip_runtime.h>
#include <hip/hip_bf16.h>
#include <stdint.h>

typedef __bf16 bf16;
typedef __attribute__((ext_vector_type(8))) __bf16 bf16x8;
typedef __attribute__((ext_vector_type(4))) __bf16 bf16x4;
typedef __attribute__((ext_vector_type(4))) float f32x4;
#define LAS __attribute__((address_space(3)))
#define DI __device__ __forceinline__

#define STAMP(k,i) do {} while (0)
constexpr int SEQ = 2048, DM = 448, NQKV = 1344;

DI float bfr(float x) { return (float)(__bf16)x; }

typedef __attribute__((ext_vector_type(2))) __bf16 bf16x2;
DI f32x4 bfr4(f32x4 v) {
  bf16x2 a = {(__bf16)v[0], (__bf16)v[1]}, b = {(__bf16)v[2], (__bf16)v[3]};
  unsigned ua = __builtin_bit_cast(unsigned, a), ub = __builtin_bit_cast(unsigned, b);
  asm volatile("" : "+v"(ua), "+v"(ub));
  return f32x4{__uint_as_float(ua << 16), __uint_as_float(ua & 0xffff0000u), __uint_as_float(ub << 16), __uint_as_float(ub & 0xffff0000u)};
}
DI void glds16(const void* gsrc, unsigned lds_dst) {
  unsigned keep;
  asm volatile("s_mov_b32 %0, m0\n\ts_mov_b32 m0, %2\n\ts_nop 0\n\tglobal_load_lds_dwordx4 %1, off\n\ts_mov_b32 m0, %0"
               : "=&s"(keep) : "v"(gsrc), "s"(lds_dst) : "memory");
}
#define WAIT_VM(n) asm volatile("s_waitcnt vmcnt(" #n ")" ::: "memory")
#define BARRIER() do { asm volatile("s_waitcnt lgkmcnt(0)" ::: "memory"); __builtin_amdgcn_s_barrier(); asm volatile("" ::: "memory"); } while (0)

DI bf16x8 tr_frag(const char* p, int hi_off) {
  bf16x4 lo = __builtin_amdgcn_ds_read_tr16_b64_v4bf16((LAS bf16x4*)(p));
  bf16x4 hi = __builtin_amdgcn_ds_read_tr16_b64_v4bf16((LAS bf16x4*)(p + hi_off));
  return __builtin_shufflevector(lo, hi, 0, 1, 2, 3, 4, 5, 6, 7);
}
DI f32x4 mfma16(bf16x8 a, bf16x8 b, f32x4 c) { return __builtin_amdgcn_mfma_f32_16x16x32_bf16(a, b, c, 0, 0, 0); }

DI bf16x8 pack8(f32x4 a, f32x4 b) {
  bf16x8 r;
#pragma unroll
  for (int i = 0; i < 4; ++i) { r[i] = (__bf16)a[i]; r[4 + i] = (__bf16)b[i]; }
  return r;
}
DI float phi_f(float t) {
  float r = t > 0.f ? t + 1.0f : bfr(__expf(t)) + 1e-6f;
  return bfr(r);
}
DI f32x4 phi4(f32x4 acc) {
  f32x4 t = bfr4(acc), e;
#pragma unroll
  for (int j = 0; j < 4; ++j) e[j] = t[j] > 0.f ? t[j] + 1.0f : __expf(t[j]) + 1e-6f;
  return e;
}
DI float xmax16_32(float m) {
  unsigned u = __float_as_uint(m);
  auto r = __builtin_amdgcn_permlane16_swap(u, u, false, false);
  float a = fmaxf(__uint_as_float(r[0]), __uint_as_float(r[1]));
  unsigned v = __float_as_uint(a);
  auto r2 = __builtin_amdgcn_permlane32_swap(v, v, false, false);
  return fmaxf(__uint_as_float(r2[0]), __uint_as_float(r2[1]));
}
DI float xsum16_32(float m) {
  unsigned u = __float_as_uint(m);
  auto r = __builtin_amdgcn_permlane16_swap(u, u, false, false);
  float a = __uint_as_float(r[0]) + __uint_as_float(r[1]);
  unsigned v = __float_as_uint(a);
  auto r2 = __builtin_amdgcn_permlane32_swap(v, v, false, false);
  return __uint_as_float(r2[0]) + __uint_as_float(r2[1]);
}
DI float bcast_row3(float x) {
  unsigned u = __float_as_uint(x);
  auto r = __builtin_amdgcn_permlane32_swap(u, u, false, false);
  unsigned v = r[1];
  auto r2 = __builtin_amdgcn_permlane16_swap(v, v, false, false);
  return __uint_as_float(r2[1]);
}
template <int NM, int ND> DI void interleave() {
  constexpr int LEAD = ND > NM ? ND - NM : 0;
  if (LEAD) __builtin_amdgcn_sched_group_barrier(0x100, LEAD, 0);
#pragma unroll
  for (int i = 0; i < NM; ++i) {
    __builtin_amdgcn_sched_group_barrier(0x008, 1, 0);
    if (i + LEAD < ND) __builtin_amdgcn_sched_group_barrier(0x100, 1, 0);
  }
}
DI int pi_inv(int lr) { return lr < 16 ? 8 * (lr >> 2) + (lr & 3) : 8 * ((lr - 16) >> 2) + 4 + (lr & 3); }

constexpr int K1_XB = 144 * 128;
constexpr int K1_WB = 64 * 352;
constexpr int K1_STAGE = K1_XB + K1_WB;
constexpr int K1_LDS = 3 * K1_STAGE;

DI void k1_loader(const bf16* __restrict__ x, const bf16* __restrict__ w, int rt, int hg, char* lds, int wv, int lane) {
  const int s0 = rt * 128;
  const unsigned ldsbase = (unsigned)(size_t)lds;
  const char* src[10];
  unsigned dst[10];
  long strd0;
#pragma unroll
  for (int i = 0; i < 4; ++i) {
    int xi = 2 + wv + 4 * i;
    int row = xi * 8 + (lane >> 3), pos = lane & 7;
    int grow = s0 - 16 + row;
    src[i] = (const char*)(x + (long)grow * DM + ((pos ^ (row & 7)) * 8));
    dst[i] = xi * 1024;
  }
#pragma unroll
  for (int i = 0; i < 6; ++i) {
    int j = wv + 4 * i;
    if (i == 0 && j < 2) {
      int row = j * 8 + (lane >> 3), pos = lane & 7;
      int grow = s0 - 16 + row; grow = grow < 0 ? 0 : grow;
      src[4] = (const char*)(x + (long)grow * DM + ((pos ^ (row & 7)) * 8));
      dst[4] = j * 1024;
      strd0 = 128;
    } else {
      int wi = j - 2;
      int c = wi * 64 + lane;
      int lrow = c / 22, pos = c - lrow * 22;
      pos = pos > 20 ? 20 : pos;
      int k = (lrow & 32) + pi_inv(lrow & 31);
      int seg = pos / 7, cc = pos - seg * 7;
      src[4 + i] = (const char*)(w + (long)k * NQKV + seg * DM + hg * 56 + cc * 8);
      dst[4 + i] = K1_XB + wi * 1024;
      if (i == 0) strd0 = 64L * NQKV * 2;
    }
  }
  auto issue = [&](int st, int buf) {
    unsigned b = ldsbase + buf * K1_STAGE;
#pragma unroll
    for (int i = 0; i < 4; ++i) glds16(src[i] + st * 128, b + dst[i]);
    glds16(src[4] + st * strd0, b + dst[4]);
#pragma unroll
    for (int i = 5; i < 10; ++i) glds16(src[i] + (long)st * (64L * NQKV * 2), b + dst[i]);
  };
  issue(0, 0);
  issue(1, 1);
#pragma unroll 1
  for (int st = 0; st < 7; ++st) {
    if (st < 6) WAIT_VM(10); else WAIT_VM(0);
    __builtin_amdgcn_s_barrier();
    if (st + 2 < 7) issue(st + 2, (st + 2) % 3);
  }
}

template <int BR>
DI void k1_body(bf16* __restrict__ outq, float* __restrict__ part, bf16* __restrict__ dummy, int rt, int hg, char* lds,
                int wv, int lane) {
  constexpr int NT = 4;
  const int rq = wv >> 1, hh = wv & 1;
  const int g = lane >> 4, c16 = lane & 15;
  const int s0 = rt * 128;

  constexpr int T0 = BR ? 0 : 1;
  f32x4 aq[NT][2], ak[NT + 1][2], av[NT + 1][2];
#pragma unroll
  for (int t = 0; t < NT + 1; ++t)
#pragma unroll
    for (int c = 0; c < 2; ++c) {
      if (t < NT) aq[t][c] = f32x4{0, 0, 0, 0};
      ak[t][c] = f32x4{0, 0, 0, 0}; av[t][c] = f32x4{0, 0, 0, 0};
    }

  STAMP(0,0);
  const int xoff = (rq * 64 + c16) * 128;
  const int woff = K1_XB + (4 * g + (c16 >> 2)) * 352 + hh * 56 + (c16 & 3) * 8;
  struct Frags { bf16x8 xf[NT + 1], wf[6]; };
  auto ldf = [&](Frags& f, const char* sb, int ks) {
    const int xs = ((ks * 4 + g) ^ (c16 & 7)) << 4;
#pragma unroll
    for (int ct = 0; ct < 6; ++ct)
      f.wf[ct] = tr_frag(sb + woff + ks * 32 * 352 + (ct >> 1) * 112 + (ct & 1) * 32, 16 * 352);
#pragma unroll
    for (int t = T0; t < NT + 1; ++t) f.xf[t] = *(const bf16x8*)(sb + xoff + t * 2048 + xs);
  };
  auto mma = [&](const Frags& f) {
#pragma unroll
    for (int t = T0; t < NT + 1; ++t) {
#pragma unroll
      for (int c = 0; c < 2; ++c) {
        if (t >= 1) aq[t - 1][c] = mfma16(f.wf[c], f.xf[t], aq[t - 1][c]);
        if (BR) ak[t][c] = mfma16(f.wf[2 + c], f.xf[t], ak[t][c]);
        else    ak[t][c] = mfma16(f.xf[t], f.wf[2 + c], ak[t][c]);
        av[t][c] = mfma16(f.xf[t], f.wf[4 + c], av[t][c]);
      }
    }
  };
  Frags fa, fb;
  BARRIER();
  STAMP(0,2);
  ldf(fa, lds, 0);
  int buf = 0;
#pragma unroll 1
  for (int st = 0; st < 6; ++st) {
    if (st == 3) STAMP(0,3);
    ldf(fb, lds + buf * K1_STAGE, 1);
    mma(fa);
    interleave<BR ? 28 : 24, BR ? 17 : 16>();
    __builtin_amdgcn_sched_barrier(0);
    buf = buf == 2 ? 0 : buf + 1;
    BARRIER();
    ldf(fa, lds + buf * K1_STAGE, 0);
    mma(fb);
    interleave<BR ? 28 : 24, BR ? 17 : 16>();
    __builtin_amdgcn_sched_barrier(0);
  }
  ldf(fb, lds + buf * K1_STAGE, 1);
  mma(fa);
  interleave<BR ? 28 : 24, BR ? 17 : 16>();
  __builtin_amdgcn_sched_barrier(0);
  mma(fb);

  STAMP(0,4);
  const int h = hg * 2 + hh;
  const int srow = s0 + rq * 64 + c16;
  if (BR) {
    const float scale = 0.18898223650461362f;
    bf16x8 kp[NT + 1], qp[NT];
#pragma unroll
    for (int t = 0; t < NT + 1; ++t) {
      f32x4 hi = ak[t][1];
      if (g == 3) hi = f32x4{0, 0, 0, 0};
      kp[t] = pack8(ak[t][0], hi);
    }
#pragma unroll
    for (int t = 0; t < NT; ++t) {
      f32x4 hi = aq[t][1];
      if (g == 3) hi = f32x4{0, 0, 0, 0};
      qp[t] = pack8(aq[t][0], hi);
    }
    f32x4 sp[NT], sc[NT];
#pragma unroll
    for (int t = 0; t < NT; ++t) {
      sp[t] = mfma16(kp[t], qp[t], f32x4{0, 0, 0, 0});
      sc[t] = mfma16(kp[t + 1], qp[t], f32x4{0, 0, 0, 0});
    }
    bool okp[4], okc[4];
#pragma unroll
    for (int j = 0; j < 4; ++j) {
      int dp = c16 + 16 - 4 * g - j, dc = c16 - 4 * g - j;
      okp[j] = ((dp & 1) == 0) && dp <= 14;
      okc[j] = dc >= 0 && ((dc & 1) == 0);
    }
    const bool first = (s0 + rq * 64) == 0;
    const float NEG = -__builtin_inff();
    float m[NT];
#pragma unroll
    for (int t = 0; t < NT; ++t) {
#pragma unroll
      for (int j = 0; j < 4; ++j) {
        bool op = okp[j] && !(t == 0 && first);
        sp[t][j] = op ? sp[t][j] : NEG;
        sc[t][j] = okc[j] ? sc[t][j] : NEG;
      }
      m[t] = fmaxf(fmaxf(fmaxf(sp[t][0], sp[t][1]), fmaxf(sp[t][2], sp[t][3])),
                   fmaxf(fmaxf(sc[t][0], sc[t][1]), fmaxf(sc[t][2], sc[t][3])));
    }
#pragma unroll
    for (int t = 0; t < NT; ++t) m[t] = xmax16_32(m[t]);
    const float c2 = scale * 1.4426950408889634f;
    float sum[NT];
#pragma unroll
    for (int t = 0; t < NT; ++t) {
      const float mc = -m[t] * c2;
#pragma unroll
      for (int j = 0; j < 4; ++j) {
        sp[t][j] = __builtin_amdgcn_exp2f(fmaf(sp[t][j], c2, mc));
        sc[t][j] = __builtin_amdgcn_exp2f(fmaf(sc[t][j], c2, mc));
      }
      f32x4 sv = sp[t] + sc[t];
      sum[t] = (sv[0] + sv[1]) + (sv[2] + sv[3]);
    }
#pragma unroll
    for (int t = 0; t < NT; ++t) sum[t] = __builtin_amdgcn_rcpf(xsum16_32(sum[t]));
    f32x4 o[NT][2];
#pragma unroll
    for (int t = 0; t < NT; ++t) {
      bf16x8 pp = pack8(sp[t] * sum[t], sc[t] * sum[t]);
#pragma unroll
      for (int et = 0; et < 2; ++et) o[t][et] = mfma16(pack8(av[t][et], av[t + 1][et]), pp, f32x4{0, 0, 0, 0});
    }
#pragma unroll
    for (int t = 0; t < NT; ++t)
#pragma unroll
      for (int et = 0; et < 2; ++et) {
        bf16x4 ob;
#pragma unroll
        for (int j = 0; j < 4; ++j) ob[j] = (__bf16)o[t][et][j];
        bf16* dp = outq + (long)(srow + 16 * t) * DM + h * 28 + 16 * et + 4 * g;
        if (et == 1) dp = (g < 3) ? dp : dummy + lane * 4;
        *(bf16x4*)dp = ob;
      }
  } else {
#pragma unroll
    for (int t = 0; t < NT; ++t)
#pragma unroll
      for (int c = 0; c < 2; ++c) {
        bf16x4 ob;
        f32x4 pv = phi4(aq[t][c]);
#pragma unroll
        for (int j = 0; j < 4; ++j) ob[j] = (__bf16)pv[j];
        bf16* dp = outq + (long)(srow + 16 * t) * DM + h * 28 + 16 * c + 4 * g;
        if (c == 1) dp = (g < 3) ? dp : dummy + lane * 4;
        *(bf16x4*)dp = ob;
      }
#pragma unroll
    for (int t = 1; t < NT + 1; ++t) {
      ak[t][0] = phi4(ak[t][0]);
      ak[t][1] = phi4(ak[t][1]);
#pragma unroll
      for (int j = 0; j < 4; ++j)
        if (c16 >= 12) av[t][1][j] = (c16 == 12) ? 1.0f : 0.0f;
    }
    f32x4 kv[2][2];
#pragma unroll
    for (int dc = 0; dc < 2; ++dc)
#pragma unroll
      for (int ec = 0; ec < 2; ++ec)
        kv[dc][ec] = mfma16(pack8(ak[3][dc], ak[4][dc]), pack8(av[3][ec], av[4][ec]),
                            mfma16(pack8(ak[1][dc], ak[2][dc]), pack8(av[1][ec], av[2][ec]), f32x4{0, 0, 0, 0}));
    BARRIER();
    f32x4* ex = (f32x4*)(lds) + hh * 1024 + lane;
    if (rq != 0) {
#pragma unroll
      for (int i = 0; i < 4; ++i) ex[(rq * 4 + i) * 64] = kv[i >> 1][i & 1];
    }
    BARRIER();
    if (rq == 0) {
      float* pb = part + ((long)rt * 16 + h) * 1024;
#pragma unroll
      for (int i = 0; i < 4; ++i) {
        f32x4 o = kv[i >> 1][i & 1] + ex[(4 + i) * 64];
        *(f32x4*)(pb + (16 * (i & 1) + c16) * 32 + 16 * (i >> 1) + 4 * g) = o;
      }
    }
  }
}

__global__ __launch_bounds__(512, 2) void k1_qkv(const bf16* __restrict__ xa, const bf16* __restrict__ wa,
                                                 const bf16* __restrict__ xb, const bf16* __restrict__ wb,
                                                 bf16* __restrict__ pq, bf16* __restrict__ ob,
                                                 float* __restrict__ part, bf16* __restrict__ dummy,
                                                 unsigned* __restrict__ flags,
                                                 const bf16* __restrict__ woa, const bf16* __restrict__ wob) {
  extern __shared__ __attribute__((aligned(16))) char lds[];
  const int b = blockIdx.x;
  const int lane = threadIdx.x & 63;
  const int wv = __builtin_amdgcn_readfirstlane(threadIdx.x >> 6);
  const int xcd = b & 7, idx = b >> 3;
  const int br = xcd & 1, sub = xcd >> 1;
  const int rt = (sub >> 1) * 8 + (idx >> 2), hg = (sub & 1) * 4 + (idx & 3);
  if (wv >= 4) {
    if (wv == 4) flags[b * 64 + lane] = 0u;
    k1_loader(br ? xb : xa, br ? wb : wa, rt, hg, lds, wv - 4, lane);
    const int line = idx * 98 + (wv - 4) * 64 + lane;
    if (wv < 6 && line < (idx + 1) * 98) {
      unsigned v = *(const unsigned*)((const char*)(br ? wob : woa) + line * 128);
      asm volatile("" :: "v"(v));
    }
    return;
  }
  unsigned pf = 0;
#pragma unroll
  for (int pp = 0; pp < 2; ++pp) {
    const int L = threadIdx.x + pp * 256;
    const char* pa;
    if (L < 224) {
      const int li = idx * 224 + L;
      pa = (const char*)(br ? xb : xa) + (long)((sub >> 1) * 1024) * (DM * 2) + li * 128;
    } else {
      int wi = idx * 168 + (L - 224); wi = wi < 5376 ? wi : 5375;
      const int k = wi / 12, r = wi - k * 12;
      int off = k * (NQKV * 2) + (r >> 2) * (DM * 2) + (sub & 1) * 448 + (r & 3) * 128;
      off = off < (DM * NQKV * 2 - 4) ? off : (DM * NQKV * 2 - 4);
      pa = (const char*)(br ? wb : wa) + off;
    }
    if (L < 392) pf += *(const unsigned*)pa;
  }
  if (br == 0) k1_body<0>(pq, part, dummy, rt, hg, lds, wv, lane);
  else         k1_body<1>(ob, part, dummy, rt, hg, lds, wv, lane);
  asm volatile("" :: "v"(pf));
  STAMP(0,5);
}

constexpr int OP_OB = 7 * 64 * 128;
constexpr int OP_WB = 448 * 224;
constexpr int OP_LDS = OP_OB + OP_WB;

DI void op_issue_a(const bf16* __restrict__ a, int m0, int wv, int lane, unsigned ldsbase) {
  const int row = wv * 8 + (lane >> 3), pos = lane & 7;
  const int aoff = ((m0 + row) * DM + ((pos ^ (row & 7)) * 8)) * 2;
#pragma unroll
  for (int kc = 0; kc < 7; ++kc) glds16((const char*)a + aoff + kc * 128, ldsbase + kc * 8192 + wv * 1024);
}
template <int NW>
DI void op_issue_w(const bf16* __restrict__ wo, int n0, int wv, int lane, unsigned ldsbase) {
  int woffs[NW];
#pragma unroll
  for (int i = 0; i < NW; ++i) {
    int c = (wv + 8 * i) * 64 + lane;
    int lrow = c / 14, pos = c - lrow * 14;
    woffs[i] = (((lrow & 32) + pi_inv(lrow & 31)) * DM + n0 + pos * 8) * 2;
  }
#pragma unroll
  for (int kc = 0; kc < 7; ++kc)
#pragma unroll
    for (int i = 0; i < NW; ++i)
      glds16((const char*)wo + woffs[i] + kc * (64 * DM * 2), ldsbase + OP_OB + kc * 14336 + (wv + 8 * i) * 1024);
}

template <int NC>
DI void op_compute(const char* lds, bf16* __restrict__ out, int m0, int ncol0, int ct0, int rbase, int lane) {
  const int g = lane >> 4, c16 = lane & 15;
  f32x4 acc[2][NC];
#pragma unroll
  for (int r = 0; r < 2; ++r)
#pragma unroll
    for (int c = 0; c < NC; ++c) acc[r][c] = f32x4{0, 0, 0, 0};
  const char* op = lds + (rbase + c16) * 128;
  const char* wp = lds + OP_OB + (4 * g + (c16 >> 2)) * 224 + ct0 * 32 + (c16 & 3) * 8;
  const int xs0 = (g ^ (c16 & 7)) << 4, xs1 = ((4 + g) ^ (c16 & 7)) << 4;
  struct Fr { bf16x8 of[2], wf[NC]; };
  auto ldf = [&](Fr& f, int xs, int wo) {
#pragma unroll
    for (int c = 0; c < NC; ++c) f.wf[c] = tr_frag(wp + wo + c * 32, 16 * 224);
#pragma unroll
    for (int r = 0; r < 2; ++r) f.of[r] = *(const bf16x8*)(op + r * 2048 + xs);
  };
  auto mma = [&](const Fr& f) {
#pragma unroll
    for (int r = 0; r < 2; ++r)
#pragma unroll
      for (int c = 0; c < NC; ++c) acc[r][c] = mfma16(f.wf[c], f.of[r], acc[r][c]);
  };
  Fr fa, fb;
  ldf(fa, xs0, 0);
#pragma unroll 1
  for (int kc = 0; kc < 6; ++kc) {
    ldf(fb, xs1, 32 * 224);
    mma(fa);
    __builtin_amdgcn_sched_barrier(0);
    op += 8192; wp += 64 * 224;
    ldf(fa, xs0, 0);
    mma(fb);
    __builtin_amdgcn_sched_barrier(0);
  }
  ldf(fb, xs1, 32 * 224);
  mma(fa);
  mma(fb);
#pragma unroll
  for (int r = 0; r < 2; ++r)
#pragma unroll
    for (int c = 0; c < NC; ++c) {
      bf16x4 ob;
#pragma unroll
      for (int j = 0; j < 4; ++j) ob[j] = (__bf16)acc[r][c][j];
      *(bf16x4*)(out + (long)(m0 + rbase + r * 16 + c16) * (2 * DM) + ncol0 + (ct0 + c) * 16 + 4 * g) = ob;
    }
}

#define RLX_AGENT __ATOMIC_RELAXED, __HIP_MEMORY_SCOPE_AGENT
typedef __attribute__((address_space(1))) unsigned gu32;
typedef __attribute__((address_space(1))) unsigned long long gu64;

DI bf16x4 reduce_head(const float* __restrict__ part, int h, int tid) {
  const float* p = part + (long)h * 1024 + tid * 4;
  f32x4 v[16];
#pragma unroll
  for (int i = 0; i < 16; ++i) v[i] = *(const f32x4*)(p + (long)i * 16 * 1024);
  f32x4 s = v[0];
#pragma unroll
  for (int i = 1; i < 16; ++i) s += v[i];
  const int e = tid >> 3, d = (tid & 7) * 4;
  bf16x4 r;
#pragma unroll
  for (int j = 0; j < 4; ++j) r[j] = (__bf16)((e <= 28 && d + j < 28) ? s[j] : 0.f);
  return r;
}

__global__ __launch_bounds__(512, 2) void k2_out(const bf16* __restrict__ pq, const bf16* __restrict__ woa,
                                                 const bf16* __restrict__ ob, const bf16* __restrict__ wob,
                                                 const float* __restrict__ part, bf16* kvt,
                                                 unsigned* flags, bf16* slow, bf16* __restrict__ out) {
  extern __shared__ __attribute__((aligned(16))) char lds[];
  const int tid = threadIdx.x, lane = tid & 63;
  const int wv = __builtin_amdgcn_readfirstlane(tid >> 6);
  const int g = lane >> 4, c16 = lane & 15;
  const int b = blockIdx.x;
  const int xcd = b & 7, idx = b >> 3;
  const int br = xcd & 1, sub = xcd >> 1;
  const int mt = sub * 8 + (idx >> 2), nt = idx & 3;
  const int m0 = mt * 64, n0 = nt * 112;
  const unsigned ldsbase = (unsigned)(size_t)lds;
  STAMP(1,0);
  if (br == 1) {
    if (idx < 4 && wv < 4) {
      __builtin_amdgcn_s_setprio(3);
      const int h = sub * 4 + idx;
      bf16x4 r = reduce_head(part, h, tid);
      const int e = tid >> 3, d = (tid & 7) * 4;
      const unsigned long long v = __builtin_bit_cast(unsigned long long, r);
      gu64* gp = (gu64*)flags + (((h * 2 + (e >> 4)) * 4 + ((d & 7) >> 1)) * 64 + (d >> 3) * 16 + (e & 15));
      __hip_atomic_store(gp, (1ull << 32) | (v & 0xffffffffull), RLX_AGENT);
      __hip_atomic_store(gp + 64, (1ull << 32) | (v >> 32), RLX_AGENT);
    }
    op_issue_a(ob, m0, wv, lane, ldsbase);
    if (wv < 6) op_issue_w<2>(wob, n0, wv, lane, ldsbase);
    else        op_issue_w<1>(wob, n0, wv, lane, ldsbase);
    STAMP(1,1);
  } else {
  op_issue_a(pq, m0, wv, lane, ldsbase);
  if (wv < 6) { op_issue_w<2>(woa, n0, wv, lane, ldsbase); WAIT_VM(14); }
  else        { op_issue_w<1>(woa, n0, wv, lane, ldsbase); WAIT_VM(7); }
  BARRIER();
  STAMP(1,1);
  bf16x8 qf[2][4];
#pragma unroll
  for (int hi = 0; hi < 2; ++hi)
#pragma unroll
    for (int t = 0; t < 4; ++t) {
      const int row = 16 * t + c16;
      const int col = (wv * 2 + hi) * 28 + 8 * g, col1 = col + 4;
      bf16x4 lo = *(const bf16x4*)(lds + (col >> 6) * 8192 + row * 128 + ((((col & 63) >> 3) ^ (row & 7)) << 4) + (col & 7) * 2);
      int a1 = (col1 >> 6) * 8192 + row * 128 + ((((col1 & 63) >> 3) ^ (row & 7)) << 4) + (col1 & 7) * 2;
      bf16x4 hi4 = *(const bf16x4*)(lds + a1);
      if (g == 3) { hi4[0] = (__bf16)0.f; hi4[1] = (__bf16)0.f; hi4[2] = (__bf16)0.f; hi4[3] = (__bf16)0.f; }
      qf[hi][t] = __builtin_shufflevector(lo, hi4, 0, 1, 2, 3, 4, 5, 6, 7);
    }
  bf16x8 kf[2][2];
  {
    const gu64* gp = (const gu64*)flags + (wv * 16) * 64 + lane;
    unsigned val[16];
    bool okall = false;
    for (int spin = 0; spin < 4000; ++spin) {
      bool ok = true;
#pragma unroll
      for (int i = 0; i < 16; ++i) {
        unsigned long long x = __hip_atomic_load(gp + i * 64, RLX_AGENT);
        val[i] = (unsigned)x;
        ok = ok && ((x >> 32) == 1ull);
      }
      if (__all(ok)) { okall = true; break; }

    }
    typedef __attribute__((ext_vector_type(4))) unsigned u32x4;
    if (okall) {
#pragma unroll
      for (int q = 0; q < 4; ++q)
        kf[q >> 1][q & 1] = __builtin_bit_cast(bf16x8, u32x4{val[4 * q], val[4 * q + 1], val[4 * q + 2], val[4 * q + 3]});
    } else {
      bf16* mine = slow + (long)b * 16384;
#pragma unroll 1
      for (int i = 0; i < 8; ++i) {
        const int h = wv * 2 + (i >> 2), t4 = (i & 3) * 64 + lane;
        *(bf16x4*)(mine + h * 1024 + t4 * 4) = reduce_head(part, h, t4);
      }
      asm volatile("s_waitcnt vmcnt(0)" ::: "memory");
#pragma unroll
      for (int hi = 0; hi < 2; ++hi) {
        kf[hi][0] = *(const bf16x8*)(mine + (wv * 2 + hi) * 1024 + c16 * 32 + 8 * g);
        kf[hi][1] = *(const bf16x8*)(mine + (wv * 2 + hi) * 1024 + (16 + c16) * 32 + 8 * g);
      }
    }
  }
  STAMP(1,2);
#pragma unroll
  for (int hi = 0; hi < 2; ++hi) {
    const int h = wv * 2 + hi;
    f32x4 n0v[4], n1v[4];
#pragma unroll
    for (int t = 0; t < 4; ++t) {
      n0v[t] = mfma16(kf[hi][0], qf[hi][t], f32x4{0, 0, 0, 0});
      n1v[t] = mfma16(kf[hi][1], qf[hi][t], f32x4{0, 0, 0, 0});
    }
#pragma unroll
    for (int t = 0; t < 4; ++t) {
      const float inv = __builtin_amdgcn_rcpf(bcast_row3(n1v[t][0]) + 1e-6f);
      bf16x4 o0, o1;
      f32x4 r0 = n0v[t] * inv, r1 = n1v[t] * inv;
#pragma unroll
      for (int j = 0; j < 4; ++j) { o0[j] = (__bf16)r0[j]; o1[j] = (__bf16)r1[j]; }
      const int row = 16 * t + c16;
      const int col = h * 28 + 4 * g;
      *(bf16x4*)(lds + (col >> 6) * 8192 + row * 128 + ((((col & 63) >> 3) ^ (row & 7)) << 4) + (col & 7) * 2) = o0;
      const int col1 = col + 16;
      int a1 = (col1 >> 6) * 8192 + row * 128 + ((((col1 & 63) >> 3) ^ (row & 7)) << 4) + (col1 & 7) * 2;
      a1 = (g < 3) ? a1 : OP_LDS + 16 + lane * 8;
      *(bf16x4*)(lds + a1) = o1;
    }
  }
  }
  asm volatile("s_waitcnt vmcnt(0)" ::: "memory");
  BARRIER();
  STAMP(1,3);
  if (wv < 6) op_compute<2>(lds, out, m0, br * DM + n0, (wv >> 1) * 2, (wv & 1) * 32, lane);
  else        op_compute<1>(lds, out, m0, br * DM + n0, 6, (wv & 1) * 32, lane);
  STAMP(1,4);
}

extern "C" void kernel_launch(void* const* d_in, const int* in_sizes, int n_in,
                              void* d_out, int out_size, void* d_ws, size_t ws_size,
                              hipStream_t stream) {
  const bf16* xa = (const bf16*)d_in[0];
  const bf16* wqa = (const bf16*)d_in[1];
  const bf16* woa = (const bf16*)d_in[2];
  const bf16* xb = (const bf16*)d_in[3];
  const bf16* wqb = (const bf16*)d_in[4];
  const bf16* wob = (const bf16*)d_in[5];
  bf16* out = (bf16*)d_out;
  char* ws = (char*)d_ws;
  bf16* pq = (bf16*)ws;
  bf16* ob = (bf16*)(ws + 2 * 1024 * 1024);
  float* part = (float*)(ws + 4 * 1024 * 1024);
  bf16* kvt = (bf16*)(ws + 6 * 1024 * 1024);
  unsigned* flags = (unsigned*)(ws + 7 * 1024 * 1024);
  bf16* dummy = (bf16*)(ws + 7 * 1024 * 1024 + 131072);
  bf16* slow = (bf16*)(ws + 8 * 1024 * 1024);

  (void)hipFuncSetAttribute((const void*)k1_qkv, hipFuncAttributeMaxDynamicSharedMemorySize, K1_LDS);
  (void)hipFuncSetAttribute((const void*)k2_out, hipFuncAttributeMaxDynamicSharedMemorySize, OP_LDS + 1024);

  k1_qkv<<<256, 512, K1_LDS, stream>>>(xa, wqa, xb, wqb, pq, ob, part, dummy, flags, woa, wob);
  k2_out<<<256, 512, OP_LDS + 1024, stream>>>(pq, woa, ob, wob, part, kvt, flags, slow, out);
}
```

```cpp
#include <hip/hip_runtime.h>
#include <hip/hip_bf16.h>
#include <stdint.h>

typedef __bf16 bf16;
typedef __attribute__((ext_vector_type(8))) __bf16 bf16x8;
typedef __attribute__((ext_vector_type(4))) __bf16 bf16x4;
typedef __attribute__((ext_vector_type(4))) float f32x4;
#define LAS __attribute__((address_space(3)))
#define DI __device__ __forceinline__

constexpr int SEQ = 2048, DM = 448, NQKV = 1344;

DI float bfr(float x) { return (float)(__bf16)x; }

typedef __attribute__((ext_vector_type(2))) __bf16 bf16x2;
DI f32x4 bfr4(f32x4 v) {
  bf16x2 a = {(__bf16)v[0], (__bf16)v[1]}, b = {(__bf16)v[2], (__bf16)v[3]};
  unsigned ua = __builtin_bit_cast(unsigned, a), ub = __builtin_bit_cast(unsigned, b);
  asm volatile("" : "+v"(ua), "+v"(ub));
  return f32x4{__uint_as_float(ua << 16), __uint_as_float(ua & 0xffff0000u), __uint_as_float(ub << 16), __uint_as_float(ub & 0xffff0000u)};
}
DI void glds16(const void* gsrc, unsigned lds_dst) {
  unsigned keep;
  asm volatile("s_mov_b32 %0, m0\n\ts_mov_b32 m0, %2\n\ts_nop 0\n\tglobal_load_lds_dwordx4 %1, off\n\ts_mov_b32 m0, %0"
               : "=&s"(keep) : "v"(gsrc), "s"(lds_dst) : "memory");
}
#define WAIT_VM(n) asm volatile("s_waitcnt vmcnt(" #n ")" ::: "memory")
#define BARRIER() do { asm volatile("s_waitcnt lgkmcnt(0)" ::: "memory"); __builtin_amdgcn_s_barrier(); asm volatile("" ::: "memory"); } while (0)

DI bf16x8 tr_frag(const char* p, int hi_off) {
  bf16x4 lo = __builtin_amdgcn_ds_read_tr16_b64_v4bf16((LAS bf16x4*)(p));
  bf16x4 hi = __builtin_amdgcn_ds_read_tr16_b64_v4bf16((LAS bf16x4*)(p + hi_off));
  return __builtin_shufflevector(lo, hi, 0, 1, 2, 3, 4, 5, 6, 7);
}
DI f32x4 mfma16(bf16x8 a, bf16x8 b, f32x4 c) { return __builtin_amdgcn_mfma_f32_16x16x32_bf16(a, b, c, 0, 0, 0); }

DI bf16x8 pack8(f32x4 a, f32x4 b) {
  bf16x8 r;
#pragma unroll
  for (int i = 0; i < 4; ++i) { r[i] = (__bf16)a[i]; r[4 + i] = (__bf16)b[i]; }
  return r;
}
DI f32x4 phi4(f32x4 acc) {
  f32x4 t = bfr4(acc), e;
#pragma unroll
  for (int j = 0; j < 4; ++j) e[j] = t[j] > 0.f ? t[j] + 1.0f : __expf(t[j]) + 1e-6f;
  return e;
}
DI float xmax16_32(float m) {
  unsigned u = __float_as_uint(m);
  auto r = __builtin_amdgcn_permlane16_swap(u, u, false, false);
  float a = fmaxf(__uint_as_float(r[0]), __uint_as_float(r[1]));
  unsigned v = __float_as_uint(a);
  auto r2 = __builtin_amdgcn_permlane32_swap(v, v, false, false);
  return fmaxf(__uint_as_float(r2[0]), __uint_as_float(r2[1]));
}
DI float xsum16_32(float m) {
  unsigned u = __float_as_uint(m);
  auto r = __builtin_amdgcn_permlane16_swap(u, u, false, false);
  float a = __uint_as_float(r[0]) + __uint_as_float(r[1]);
  unsigned v = __float_as_uint(a);
  auto r2 = __builtin_amdgcn_permlane32_swap(v, v, false, false);
  return __uint_as_float(r2[0]) + __uint_as_float(r2[1]);
}
DI float bcast_row3(float x) {
  unsigned u = __float_as_uint(x);
  auto r = __builtin_amdgcn_permlane32_swap(u, u, false, false);
  unsigned v = r[1];
  auto r2 = __builtin_amdgcn_permlane16_swap(v, v, false, false);
  return __uint_as_float(r2[1]);
}
template <int NM, int ND> DI void interleave() {
  constexpr int LEAD = ND > NM ? ND - NM : 0;
  if (LEAD) __builtin_amdgcn_sched_group_barrier(0x100, LEAD, 0);
#pragma unroll
  for (int i = 0; i < NM; ++i) {
    __builtin_amdgcn_sched_group_barrier(0x008, 1, 0);
    if (i + LEAD < ND) __builtin_amdgcn_sched_group_barrier(0x100, 1, 0);
  }
}
DI int pi_inv(int lr) { return lr < 16 ? 8 * (lr >> 2) + (lr & 3) : 8 * ((lr - 16) >> 2) + 4 + (lr & 3); }

constexpr int K1_XB = 144 * 128;
constexpr int K1_WB = 64 * 352;
constexpr int K1_STAGE = K1_XB + K1_WB;
constexpr int K1_LDS = 3 * K1_STAGE;

DI void k1_loader(const bf16* __restrict__ x, const bf16* __restrict__ w, int rt, int hg, char* lds, int wv, int lane) {
  const int s0 = rt * 128;
  const unsigned ldsbase = (unsigned)(size_t)lds;
  const char* src[10];
  unsigned dst[10];
  long strd0;
#pragma unroll
  for (int i = 0; i < 4; ++i) {
    int xi = 2 + wv + 4 * i;
    int row = xi * 8 + (lane >> 3), pos = lane & 7;
    int grow = s0 - 16 + row;
    src[i] = (const char*)(x + (long)grow * DM + ((pos ^ (row & 7)) * 8));
    dst[i] = xi * 1024;
  }
#pragma unroll
  for (int i = 0; i < 6; ++i) {
    int j = wv + 4 * i;
    if (i == 0 && j < 2) {
      int row = j * 8 + (lane >> 3), pos = lane & 7;
      int grow = s0 - 16 + row; grow = grow < 0 ? 0 : grow;
      src[4] = (const char*)(x + (long)grow * DM + ((pos ^ (row & 7)) * 8));
      dst[4] = j * 1024;
      strd0 = 128;
    } else {
      int wi = j - 2;
      int c = wi * 64 + lane;
      int lrow = c / 22, pos = c - lrow * 22;
      pos = pos > 20 ? 20 : pos;
      int k = (lrow & 32) + pi_inv(lrow & 31);
      int seg = pos / 7, cc = pos - seg * 7;
      src[4 + i] = (const char*)(w + (long)k * NQKV + seg * DM + hg * 56 + cc * 8);
      dst[4 + i] = K1_XB + wi * 1024;
      if (i == 0) strd0 = 64L * NQKV * 2;
    }
  }
  auto issue = [&](int st, int buf) {
    unsigned b = ldsbase + buf * K1_STAGE;
#pragma unroll
    for (int i = 0; i < 4; ++i) glds16(src[i] + st * 128, b + dst[i]);
    glds16(src[4] + st * strd0, b + dst[4]);
#pragma unroll
    for (int i = 5; i < 10; ++i) glds16(src[i] + (long)st * (64L * NQKV * 2), b + dst[i]);
  };
  issue(0, 0);
  issue(1, 1);
#pragma unroll 1
  for (int st = 0; st < 7; ++st) {
    if (st < 6) WAIT_VM(10); else WAIT_VM(0);
    __builtin_amdgcn_s_barrier();
    if (st + 2 < 7) issue(st + 2, (st + 2) % 3);
  }
}

template <int BR>
DI void k1_body(bf16* __restrict__ outq, float* __restrict__ part, bf16* __restrict__ dummy, int rt, int hg, char* lds,
                int wv, int lane) {
  constexpr int NT = 4;
  const int rq = wv >> 1, hh = wv & 1;
  const int g = lane >> 4, c16 = lane & 15;
  const int s0 = rt * 128;

  constexpr int T0 = BR ? 0 : 1;
  f32x4 aq[NT][2], ak[NT + 1][2], av[NT + 1][2];
#pragma unroll
  for (int t = 0; t < NT + 1; ++t)
#pragma unroll
    for (int c = 0; c < 2; ++c) {
      if (t < NT) aq[t][c] = f32x4{0, 0, 0, 0};
      ak[t][c] = f32x4{0, 0, 0, 0}; av[t][c] = f32x4{0, 0, 0, 0};
    }
  const int xoff = (rq * 64 + c16) * 128;
  const int woff = K1_XB + (4 * g + (c16 >> 2)) * 352 + hh * 56 + (c16 & 3) * 8;
  struct Frags { bf16x8 xf[NT + 1], wf[6]; };
  auto ldf = [&](Frags& f, const char* sb, int ks) {
    const int xs = ((ks * 4 + g) ^ (c16 & 7)) << 4;
#pragma unroll
    for (int ct = 0; ct < 6; ++ct)
      f.wf[ct] = tr_frag(sb + woff + ks * 32 * 352 + (ct >> 1) * 112 + (ct & 1) * 32, 16 * 352);
#pragma unroll
    for (int t = T0; t < NT + 1; ++t) f.xf[t] = *(const bf16x8*)(sb + xoff + t * 2048 + xs);
  };
  auto mma = [&](const Frags& f) {
#pragma unroll
    for (int t = T0; t < NT + 1; ++t) {
#pragma unroll
      for (int c = 0; c < 2; ++c) {
        if (t >= 1) aq[t - 1][c] = mfma16(f.wf[c], f.xf[t], aq[t - 1][c]);
        if (BR) ak[t][c] = mfma16(f.wf[2 + c], f.xf[t], ak[t][c]);
        else    ak[t][c] = mfma16(f.xf[t], f.wf[2 + c], ak[t][c]);
        av[t][c] = mfma16(f.xf[t], f.wf[4 + c], av[t][c]);
      }
    }
  };
  Frags fa, fb;
  BARRIER();
  ldf(fa, lds, 0);
  int buf = 0;
#pragma unroll 1
  for (int st = 0; st < 6; ++st) {
    ldf(fb, lds + buf * K1_STAGE, 1);
    mma(fa);
    interleave<BR ? 28 : 24, BR ? 17 : 16>();
    __builtin_amdgcn_sched_barrier(0);
    buf = buf == 2 ? 0 : buf + 1;
    BARRIER();
    ldf(fa, lds + buf * K1_STAGE, 0);
    mma(fb);
    interleave<BR ? 28 : 24, BR ? 17 : 16>();
    __builtin_amdgcn_sched_barrier(0);
  }
  ldf(fb, lds + buf * K1_STAGE, 1);
  mma(fa);
  interleave<BR ? 28 : 24, BR ? 17 : 16>();
  __builtin_amdgcn_sched_barrier(0);
  mma(fb);
  const int h = hg * 2 + hh;
  const int srow = s0 + rq * 64 + c16;
  if (BR) {
    const float scale = 0.18898223650461362f;
    bf16x8 kp[NT + 1], qp[NT];
#pragma unroll
    for (int t = 0; t < NT + 1; ++t) {
      f32x4 hi = ak[t][1];
      if (g == 3) hi = f32x4{0, 0, 0, 0};
      kp[t] = pack8(ak[t][0], hi);
    }
#pragma unroll
    for (int t = 0; t < NT; ++t) {
      f32x4 hi = aq[t][1];
      if (g == 3) hi = f32x4{0, 0, 0, 0};
      qp[t] = pack8(aq[t][0], hi);
    }
    f32x4 sp[NT], sc[NT];
#pragma unroll
    for (int t = 0; t < NT; ++t) {
      sp[t] = mfma16(kp[t], qp[t], f32x4{0, 0, 0, 0});
      sc[t] = mfma16(kp[t + 1], qp[t], f32x4{0, 0, 0, 0});
    }
    bool okp[4], okc[4];
#pragma unroll
    for (int j = 0; j < 4; ++j) {
      int dp = c16 + 16 - 4 * g - j, dc = c16 - 4 * g - j;
      okp[j] = ((dp & 1) == 0) && dp <= 14;
      okc[j] = dc >= 0 && ((dc & 1) == 0);
    }
    const bool first = (s0 + rq * 64) == 0;
    const float NEG = -__builtin_inff();
    float m[NT];
#pragma unroll
    for (int t = 0; t < NT; ++t) {
#pragma unroll
      for (int j = 0; j < 4; ++j) {
        bool op = okp[j] && !(t == 0 && first);
        sp[t][j] = op ? sp[t][j] : NEG;
        sc[t][j] = okc[j] ? sc[t][j] : NEG;
      }
      m[t] = fmaxf(fmaxf(fmaxf(sp[t][0], sp[t][1]), fmaxf(sp[t][2], sp[t][3])),
                   fmaxf(fmaxf(sc[t][0], sc[t][1]), fmaxf(sc[t][2], sc[t][3])));
    }
#pragma unroll
    for (int t = 0; t < NT; ++t) m[t] = xmax16_32(m[t]);
    const float c2 = scale * 1.4426950408889634f;
    float sum[NT];
#pragma unroll
    for (int t = 0; t < NT; ++t) {
      const float mc = -m[t] * c2;
#pragma unroll
      for (int j = 0; j < 4; ++j) {
        sp[t][j] = __builtin_amdgcn_exp2f(fmaf(sp[t][j], c2, mc));
        sc[t][j] = __builtin_amdgcn_exp2f(fmaf(sc[t][j], c2, mc));
      }
      f32x4 sv = sp[t] + sc[t];
      sum[t] = (sv[0] + sv[1]) + (sv[2] + sv[3]);
    }
#pragma unroll
    for (int t = 0; t < NT; ++t) sum[t] = __builtin_amdgcn_rcpf(xsum16_32(sum[t]));
    f32x4 o[NT][2];
#pragma unroll
    for (int t = 0; t < NT; ++t) {
      bf16x8 pp = pack8(sp[t] * sum[t], sc[t] * sum[t]);
#pragma unroll
      for (int et = 0; et < 2; ++et) o[t][et] = mfma16(pack8(av[t][et], av[t + 1][et]), pp, f32x4{0, 0, 0, 0});
    }
#pragma unroll
    for (int t = 0; t < NT; ++t)
#pragma unroll
      for (int et = 0; et < 2; ++et) {
        bf16x4 ob;
#pragma unroll
        for (int j = 0; j < 4; ++j) ob[j] = (__bf16)o[t][et][j];
        bf16* dp = outq + (long)(srow + 16 * t) * DM + h * 28 + 16 * et + 4 * g;
        if (et == 1) dp = (g < 3) ? dp : dummy + lane * 4;
        *(bf16x4*)dp = ob;
      }
  } else {
#pragma unroll
    for (int t = 0; t < NT; ++t)
#pragma unroll
      for (int c = 0; c < 2; ++c) {
        bf16x4 ob;
        f32x4 pv = phi4(aq[t][c]);
#pragma unroll
        for (int j = 0; j < 4; ++j) ob[j] = (__bf16)pv[j];
        bf16* dp = outq + (long)(srow + 16 * t) * DM + h * 28 + 16 * c + 4 * g;
        if (c == 1) dp = (g < 3) ? dp : dummy + lane * 4;
        *(bf16x4*)dp = ob;
      }
#pragma unroll
    for (int t = 1; t < NT + 1; ++t) {
      ak[t][0] = phi4(ak[t][0]);
      ak[t][1] = phi4(ak[t][1]);
#pragma unroll
      for (int j = 0; j < 4; ++j)
        if (c16 >= 12) av[t][1][j] = (c16 == 12) ? 1.0f : 0.0f;
    }
    f32x4 kv[2][2];
#pragma unroll
    for (int dc = 0; dc < 2; ++dc)
#pragma unroll
      for (int ec = 0; ec < 2; ++ec)
        kv[dc][ec] = mfma16(pack8(ak[3][dc], ak[4][dc]), pack8(av[3][ec], av[4][ec]),
                            mfma16(pack8(ak[1][dc], ak[2][dc]), pack8(av[1][ec], av[2][ec]), f32x4{0, 0, 0, 0}));
    BARRIER();
    f32x4* ex = (f32x4*)(lds) + hh * 1024 + lane;
    if (rq != 0) {
#pragma unroll
      for (int i = 0; i < 4; ++i) ex[(rq * 4 + i) * 64] = kv[i >> 1][i & 1];
    }
    BARRIER();
    if (rq == 0) {
      float* pb = part + ((long)rt * 16 + h) * 1024;
#pragma unroll
      for (int i = 0; i < 4; ++i) {
        f32x4 o = kv[i >> 1][i & 1] + ex[(4 + i) * 64];
        *(f32x4*)(pb + (16 * (i & 1) + c16) * 32 + 16 * (i >> 1) + 4 * g) = o;
      }
    }
  }
}

__global__ __launch_bounds__(512, 2) void k1_qkv(const bf16* __restrict__ xa, const bf16* __restrict__ wa,
                                                 const bf16* __restrict__ xb, const bf16* __restrict__ wb,
                                                 bf16* __restrict__ pq, bf16* __restrict__ ob,
                                                 float* __restrict__ part, bf16* __restrict__ dummy,
                                                 unsigned* __restrict__ flags,
                                                 const bf16* __restrict__ woa, const bf16* __restrict__ wob) {
  extern __shared__ __attribute__((aligned(16))) char lds[];
  const int b = blockIdx.x;
  const int lane = threadIdx.x & 63;
  const int wv = __builtin_amdgcn_readfirstlane(threadIdx.x >> 6);
  const int xcd = b & 7, idx = b >> 3;
  const int br = xcd & 1, sub = xcd >> 1;
  const int rt = (sub >> 1) * 8 + (idx >> 2), hg = (sub & 1) * 4 + (idx & 3);
  if (wv >= 4) {
    if (wv == 4) flags[b * 64 + lane] = 0u;
    k1_loader(br ? xb : xa, br ? wb : wa, rt, hg, lds, wv - 4, lane);
    const int line = idx * 98 + (wv - 4) * 64 + lane;
    if (wv < 6 && line < (idx + 1) * 98) {
      unsigned v = *(const unsigned*)((const char*)(br ? wob : woa) + line * 128);
      asm volatile("" :: "v"(v));
    }
    return;
  }
  unsigned pf = 0;
#pragma unroll
  for (int pp = 0; pp < 2; ++pp) {
    const int L = threadIdx.x + pp * 256;
    const char* pa;
    if (L < 224) {
      const int li = idx * 224 + L;
      pa = (const char*)(br ? xb : xa) + (long)((sub >> 1) * 1024) * (DM * 2) + li * 128;
    } else {
      int wi = idx * 168 + (L - 224); wi = wi < 5376 ? wi : 5375;
      const int k = wi / 12, r = wi - k * 12;
      int off = k * (NQKV * 2) + (r >> 2) * (DM * 2) + (sub & 1) * 448 + (r & 3) * 128;
      off = off < (DM * NQKV * 2 - 4) ? off : (DM * NQKV * 2 - 4);
      pa = (const char*)(br ? wb : wa) + off;
    }
    if (L < 392) pf += *(const unsigned*)pa;
  }
  if (br == 0) k1_body<0>(pq, part, dummy, rt, hg, lds, wv, lane);
  else         k1_body<1>(ob, part, dummy, rt, hg, lds, wv, lane);
  asm volatile("" :: "v"(pf));
}

constexpr int OP_OB = 7 * 64 * 128;
constexpr int OP_WB = 448 * 224;
constexpr int OP_LDS = OP_OB + OP_WB;

DI void op_issue_a(const bf16* __restrict__ a, int m0, int wv, int lane, unsigned ldsbase) {
  const int row = wv * 8 + (lane >> 3), pos = lane & 7;
  const int aoff = ((m0 + row) * DM + ((pos ^ (row & 7)) * 8)) * 2;
#pragma unroll
  for (int kc = 0; kc < 7; ++kc) glds16((const char*)a + aoff + kc * 128, ldsbase + kc * 8192 + wv * 1024);
}
template <int NW>
DI void op_issue_w(const bf16* __restrict__ wo, int n0, int wv, int lane, unsigned ldsbase) {
  int woffs[NW];
#pragma unroll
  for (int i = 0; i < NW; ++i) {
    int c = (wv + 8 * i) * 64 + lane;
    int lrow = c / 14, pos = c - lrow * 14;
    woffs[i] = (((lrow & 32) + pi_inv(lrow & 31)) * DM + n0 + pos * 8) * 2;
  }
#pragma unroll
  for (int kc = 0; kc < 7; ++kc)
#pragma unroll
    for (int i = 0; i < NW; ++i)
      glds16((const char*)wo + woffs[i] + kc * (64 * DM * 2), ldsbase + OP_OB + kc * 14336 + (wv + 8 * i) * 1024);
}

template <int NC>
DI void op_compute(const char* lds, bf16* __restrict__ out, int m0, int ncol0, int ct0, int rbase, int lane) {
  const int g = lane >> 4, c16 = lane & 15;
  f32x4 acc[2][NC];
#pragma unroll
  for (int r = 0; r < 2; ++r)
#pragma unroll
    for (int c = 0; c < NC; ++c) acc[r][c] = f32x4{0, 0, 0, 0};
  const char* op = lds + (rbase + c16) * 128;
  const char* wp = lds + OP_OB + (4 * g + (c16 >> 2)) * 224 + ct0 * 32 + (c16 & 3) * 8;
  const int xs0 = (g ^ (c16 & 7)) << 4, xs1 = ((4 + g) ^ (c16 & 7)) << 4;
  struct Fr { bf16x8 of[2], wf[NC]; };
  auto ldf = [&](Fr& f, int xs, int wo) {
#pragma unroll
    for (int c = 0; c < NC; ++c) f.wf[c] = tr_frag(wp + wo + c * 32, 16 * 224);
#pragma unroll
    for (int r = 0; r < 2; ++r) f.of[r] = *(const bf16x8*)(op + r * 2048 + xs);
  };
  auto mma = [&](const Fr& f) {
#pragma unroll
    for (int r = 0; r < 2; ++r)
#pragma unroll
      for (int c = 0; c < NC; ++c) acc[r][c] = mfma16(f.wf[c], f.of[r], acc[r][c]);
  };
  Fr fa, fb;
  ldf(fa, xs0, 0);
#pragma unroll 1
  for (int kc = 0; kc < 6; ++kc) {
    ldf(fb, xs1, 32 * 224);
    mma(fa);
    __builtin_amdgcn_sched_barrier(0);
    op += 8192; wp += 64 * 224;
    ldf(fa, xs0, 0);
    mma(fb);
    __builtin_amdgcn_sched_barrier(0);
  }
  ldf(fb, xs1, 32 * 224);
  mma(fa);
  mma(fb);
#pragma unroll
  for (int r = 0; r < 2; ++r)
#pragma unroll
    for (int c = 0; c < NC; ++c) {
      bf16x4 ob;
#pragma unroll
      for (int j = 0; j < 4; ++j) ob[j] = (__bf16)acc[r][c][j];
      *(bf16x4*)(out + (long)(m0 + rbase + r * 16 + c16) * (2 * DM) + ncol0 + (ct0 + c) * 16 + 4 * g) = ob;
    }
}

#define RLX_AGENT __ATOMIC_RELAXED, __HIP_MEMORY_SCOPE_AGENT
typedef __attribute__((address_space(1))) unsigned gu32;
typedef __attribute__((address_space(1))) unsigned long long gu64;

DI bf16x4 reduce_head(const float* __restrict__ part, int h, int tid) {
  const float* p = part + (long)h * 1024 + tid * 4;
  f32x4 v[16];
#pragma unroll
  for (int i = 0; i < 16; ++i) v[i] = *(const f32x4*)(p + (long)i * 16 * 1024);
  f32x4 s = v[0];
#pragma unroll
  for (int i = 1; i < 16; ++i) s += v[i];
  const int e = tid >> 3, d = (tid & 7) * 4;
  bf16x4 r;
#pragma unroll
  for (int j = 0; j < 4; ++j) r[j] = (__bf16)((e <= 28 && d + j < 28) ? s[j] : 0.f);
  return r;
}

__global__ __launch_bounds__(512, 2) void k2_out(const bf16* __restrict__ pq, const bf16* __restrict__ woa,
                                                 const bf16* __restrict__ ob, const bf16* __restrict__ wob,
                                                 const float* __restrict__ part,
                                                 unsigned* flags, bf16* slow, bf16* __restrict__ out) {
  extern __shared__ __attribute__((aligned(16))) char lds[];
  const int tid = threadIdx.x, lane = tid & 63;
  const int wv = __builtin_amdgcn_readfirstlane(tid >> 6);
  const int g = lane >> 4, c16 = lane & 15;
  const int b = blockIdx.x;
  const int xcd = b & 7, idx = b >> 3;
  const int br = xcd & 1, sub = xcd >> 1;
  const int mt = sub * 8 + (idx >> 2), nt = idx & 3;
  const int m0 = mt * 64, n0 = nt * 112;
  const unsigned ldsbase = (unsigned)(size_t)lds;
  if (br == 1) {
    if (idx < 4 && wv < 4) {
      __builtin_amdgcn_s_setprio(3);
      const int h = sub * 4 + idx;
      bf16x4 r = reduce_head(part, h, tid);
      const int e = tid >> 3, d = (tid & 7) * 4;
      const unsigned long long v = __builtin_bit_cast(unsigned long long, r);
      gu64* gp = (gu64*)flags + (((h * 2 + (e >> 4)) * 4 + ((d & 7) >> 1)) * 64 + (d >> 3) * 16 + (e & 15));
      __hip_atomic_store(gp, (1ull << 32) | (v & 0xffffffffull), RLX_AGENT);
      __hip_atomic_store(gp + 64, (1ull << 32) | (v >> 32), RLX_AGENT);
    }
    op_issue_a(ob, m0, wv, lane, ldsbase);
    if (wv < 6) op_issue_w<2>(wob, n0, wv, lane, ldsbase);
    else        op_issue_w<1>(wob, n0, wv, lane, ldsbase);
  } else {
  op_issue_a(pq, m0, wv, lane, ldsbase);
  if (wv < 6) { op_issue_w<2>(woa, n0, wv, lane, ldsbase); WAIT_VM(14); }
  else        { op_issue_w<1>(woa, n0, wv, lane, ldsbase); WAIT_VM(7); }
  BARRIER();
  bf16x8 qf[2][4];
#pragma unroll
  for (int hi = 0; hi < 2; ++hi)
#pragma unroll
    for (int t = 0; t < 4; ++t) {
      const int row = 16 * t + c16;
      const int col = (wv * 2 + hi) * 28 + 8 * g, col1 = col + 4;
      bf16x4 lo = *(const bf16x4*)(lds + (col >> 6) * 8192 + row * 128 + ((((col & 63) >> 3) ^ (row & 7)) << 4) + (col & 7) * 2);
      int a1 = (col1 >> 6) * 8192 + row * 128 + ((((col1 & 63) >> 3) ^ (row & 7)) << 4) + (col1 & 7) * 2;
      bf16x4 hi4 = *(const bf16x4*)(lds + a1);
      if (g == 3) { hi4[0] = (__bf16)0.f; hi4[1] = (__bf16)0.f; hi4[2] = (__bf16)0.f; hi4[3] = (__bf16)0.f; }
      qf[hi][t] = __builtin_shufflevector(lo, hi4, 0, 1, 2, 3, 4, 5, 6, 7);
    }
  bf16x8 kf[2][2];
  {
    const gu64* gp = (const gu64*)flags + (wv * 16) * 64 + lane;
    unsigned val[16];
    bool okall = false;
    for (int spin = 0; spin < 4000; ++spin) {
      bool ok = true;
#pragma unroll
      for (int i = 0; i < 16; ++i) {
        unsigned long long x = __hip_atomic_load(gp + i * 64, RLX_AGENT);
        val[i] = (unsigned)x;
        ok = ok && ((x >> 32) == 1ull);
      }
      if (__all(ok)) { okall = true; break; }

    }
    typedef __attribute__((ext_vector_type(4))) unsigned u32x4;
    if (okall) {
#pragma unroll
      for (int q = 0; q < 4; ++q)
        kf[q >> 1][q & 1] = __builtin_bit_cast(bf16x8, u32x4{val[4 * q], val[4 * q + 1], val[4 * q + 2], val[4 * q + 3]});
    } else {
      bf16* mine = slow + (long)b * 16384;
#pragma unroll 1
      for (int i = 0; i < 8; ++i) {
        const int h = wv * 2 + (i >> 2), t4 = (i & 3) * 64 + lane;
        *(bf16x4*)(mine + h * 1024 + t4 * 4) = reduce_head(part, h, t4);
      }
      asm volatile("s_waitcnt vmcnt(0)" ::: "memory");
#pragma unroll
      for (int hi = 0; hi < 2; ++hi) {
        kf[hi][0] = *(const bf16x8*)(mine + (wv * 2 + hi) * 1024 + c16 * 32 + 8 * g);
        kf[hi][1] = *(const bf16x8*)(mine + (wv * 2 + hi) * 1024 + (16 + c16) * 32 + 8 * g);
      }
    }
  }
#pragma unroll
  for (int hi = 0; hi < 2; ++hi) {
    const int h = wv * 2 + hi;
    f32x4 n0v[4], n1v[4];
#pragma unroll
    for (int t = 0; t < 4; ++t) {
      n0v[t] = mfma16(kf[hi][0], qf[hi][t], f32x4{0, 0, 0, 0});
      n1v[t] = mfma16(kf[hi][1], qf[hi][t], f32x4{0, 0, 0, 0});
    }
#pragma unroll
    for (int t = 0; t < 4; ++t) {
      const float inv = __builtin_amdgcn_rcpf(bcast_row3(n1v[t][0]) + 1e-6f);
      bf16x4 o0, o1;
      f32x4 r0 = n0v[t] * inv, r1 = n1v[t] * inv;
#pragma unroll
      for (int j = 0; j < 4; ++j) { o0[j] = (__bf16)r0[j]; o1[j] = (__bf16)r1[j]; }
      const int row = 16 * t + c16;
      const int col = h * 28 + 4 * g;
      *(bf16x4*)(lds + (col >> 6) * 8192 + row * 128 + ((((col & 63) >> 3) ^ (row & 7)) << 4) + (col & 7) * 2) = o0;
      const int col1 = col + 16;
      int a1 = (col1 >> 6) * 8192 + row * 128 + ((((col1 & 63) >> 3) ^ (row & 7)) << 4) + (col1 & 7) * 2;
      a1 = (g < 3) ? a1 : OP_LDS + 16 + lane * 8;
      *(bf16x4*)(lds + a1) = o1;
    }
  }
  }
  asm volatile("s_waitcnt vmcnt(0)" ::: "memory");
  BARRIER();
  if (wv < 6) op_compute<2>(lds, out, m0, br * DM + n0, (wv >> 1) * 2, (wv & 1) * 32, lane);
  else        op_compute<1>(lds, out, m0, br * DM + n0, 6, (wv & 1) * 32, lane);
}

extern "C" void kernel_launch(void* const* d_in, const int* in_sizes, int n_in,
                              void* d_out, int out_size, void* d_ws, size_t ws_size,
                              hipStream_t stream) {
  const bf16* xa = (const bf16*)d_in[0];
  const bf16* wqa = (const bf16*)d_in[1];
  const bf16* woa = (const bf16*)d_in[2];
  const bf16* xb = (const bf16*)d_in[3];
  const bf16* wqb = (const bf16*)d_in[4];
  const bf16* wob = (const bf16*)d_in[5];
  bf16* out = (bf16*)d_out;
  char* ws = (char*)d_ws;
  bf16* pq = (bf16*)ws;
  bf16* ob = (bf16*)(ws + 2 * 1024 * 1024);
  float* part = (float*)(ws + 4 * 1024 * 1024);
  unsigned* flags = (unsigned*)(ws + 7 * 1024 * 1024);
  bf16* dummy = (bf16*)(ws + 7 * 1024 * 1024 + 131072);
  bf16* slow = (bf16*)(ws + 8 * 1024 * 1024);

  (void)hipFuncSetAttribute((const void*)k1_qkv, hipFuncAttributeMaxDynamicSharedMemorySize, K1_LDS);
  (void)hipFuncSetAttribute((const void*)k2_out, hipFuncAttributeMaxDynamicSharedMemorySize, OP_LDS + 1024);

  k1_qkv<<<256, 512, K1_LDS, stream>>>(xa, wqa, xb, wqb, pq, ob, part, dummy, flags, woa, wob);
  k2_out<<<256, 512, OP_LDS + 1024, stream>>>(pq, woa, ob, wob, part, flags, slow, out);
}
```
